```python
import torch

def _softmax(x, axis=-1):
    return torch.softmax(x, dim=axis)
def _gelu(x):
    return torch.nn.functional.gelu(x, approximate="tanh")
def _silu(x):
    return torch.nn.functional.silu(x)
def _sigmoid(x):
    return torch.sigmoid(x)
def _layernorm(x, axis=-1, eps=1e-5):
    m = x.mean(dim=axis, keepdim=True)
    v = x.var(dim=axis, keepdim=True, unbiased=False)
    return (x - m) / torch.sqrt(v + eps)
def _scatter_add(tgt, idx, src, axis=0):
    return tgt.clone().index_add_(axis, idx, src)
def _segment_sum(src, seg, n):
    out = torch.zeros((n,) + src.shape[1:], dtype=src.dtype)
    return out.index_add_(0, seg, src)
def _pad(x, pw):
    flat = tuple(v for pair in reversed(pw) for v in pair)
    return torch.nn.functional.pad(x, flat)

kernel_name = 'parallel_sum__gqa_block_cached__dense_activation_squared_relu__decode'

def setup_inputs():

    _w = (lambda a: (lambda t: t.to(torch.bfloat16) if t.is_floating_point() else t)(torch.as_tensor(a)))
    def _A_setup():
        B, Sq, D = 8, 1, 512
        Hq, Hkv, Sk, Dh = 32, 32, 4096, 128
        assert Hq % Hkv == 0, (Hq, Hkv)
        return {
            "x": _w((lambda *s: torch.randn(*s, dtype=torch.bfloat16) * 0.05)(B, Sq, D)),
            "k_cache": _w((lambda *s: torch.randn(*s, dtype=torch.bfloat16) * 1.6)(B, Hkv, Sk, Dh)),
            "v_cache": _w((lambda *s: torch.randn(*s, dtype=torch.bfloat16) * 3.2)(B, Hkv, Sk, Dh)),
            "wq": _w((lambda *s: torch.randn(*s, dtype=torch.bfloat16) * 1.6)(D, Hq * Dh) * (D ** -0.5)),
            "wo": _w((lambda *s: torch.randn(*s, dtype=torch.bfloat16) * 3.2)(Hq * Dh, D) * ((Hq * Dh) ** -0.5)),
        }
    _A_in = _A_setup()

    def _B_setup():
        B, S, D, F = 8, 1, 512, 1376
        return {
            "x": _w((lambda *s: torch.randn(*s, dtype=torch.bfloat16) * 0.05)(B, S, D)),
            "w1": _w((lambda *s: torch.randn(*s, dtype=torch.bfloat16) * 0.05)(D, F)),
            "b1": _w((lambda *s: torch.randn(*s, dtype=torch.bfloat16) * 0.4)(F)),
            "w2": _w((lambda *s: torch.randn(*s, dtype=torch.bfloat16) * 0.05)(F, D)),
            "b2": _w((lambda *s: torch.randn(*s, dtype=torch.bfloat16) * 1.6)(D)),
        }
    _B_in = _B_setup()

    return {
        "A_x": _A_in["x"],
        "A_k_cache": _A_in["k_cache"],
        "A_v_cache": _A_in["v_cache"],
        "A_wq": _A_in["wq"],
        "A_wo": _A_in["wo"],
        "B_x": _B_in["x"],
        "B_w1": _B_in["w1"],
        "B_b1": _B_in["b1"],
        "B_w2": _B_in["w2"],
        "B_b2": _B_in["b2"],
    }

def reference(A_x, A_k_cache, A_v_cache, A_wq, A_wo, B_x, B_w1, B_b1, B_w2, B_b2):
    def _A_ref(x, k_cache, v_cache, wq, wo):
        B, Sq, D = x.shape
        Hq, Dh = 32, 128
        Hkv = k_cache.shape[1]
        rep = Hq // Hkv
        q = (lambda x, *d: x.permute(*d))((x @ wq).reshape(B, Sq, Hq, Dh), 0, 2, 1, 3)
        k = (lambda x, n, axis: torch.repeat_interleave(x, n, dim=axis))(k_cache, rep, axis=1)
        v = (lambda x, n, axis: torch.repeat_interleave(x, n, dim=axis))(v_cache, rep, axis=1)
        scores = torch.einsum("bhqd,bhkd->bhqk", q, k) * (Dh ** -0.5)
        p = _softmax(scores, axis=-1)
        o = torch.einsum("bhqk,bhkd->bhqd", p, v)
        return (lambda x, *d: x.permute(*d))(o, 0, 2, 1, 3).reshape(B, Sq, Hq * Dh) @ wo

    def _B_ref(x, w1, b1, w2, b2):
        h = torch.einsum("bsd,df->bsf", x, w1) + b1
        zero = torch.zeros_like(h)
        r = torch.where(h > zero, h, zero)
        h = r * r
        out = torch.einsum("bsf,fd->bsd", h, w2) + b2
        return out

    x = A_x
    xn = _layernorm(x, axis=-1)
    return x + _A_ref(xn, A_k_cache, A_v_cache, A_wq, A_wo) + _B_ref(xn, B_w1, B_b1, B_w2, B_b2)
```

```cpp
#include <hip/hip_runtime.h>
#include <hip/hip_bf16.h>
#include <stdint.h>

typedef unsigned u32;
typedef u32 u32x4 __attribute__((ext_vector_type(4)));
typedef float f32x4 __attribute__((ext_vector_type(4)));
typedef __bf16 bf16x8 __attribute__((ext_vector_type(8)));
typedef __bf16 bf16x2 __attribute__((ext_vector_type(2)));

constexpr int NB = 8, DM = 512, NH = 32, SK = 4096, DH = 128, FF = 1376;
constexpr int NW = 8;
constexpr int NT = SK / 64 / NW;
constexpr float LOG2E = 1.4426950408889634f;
constexpr float SCALE = 0.08838834764831845f;

#define KV_AUX 2

__device__ __forceinline__ float bfr(float x) { return (float)(__bf16)x; }
__device__ __forceinline__ u32 bfbits(float x) { return (u32)__builtin_bit_cast(unsigned short, (__bf16)x); }
__device__ __forceinline__ u32 bfdup(float x) {
  bf16x2 v = {(__bf16)x, (__bf16)x};
  return __builtin_bit_cast(u32, v);
}
__device__ __forceinline__ float bflo(u32 v) { return __builtin_bit_cast(float, v << 16); }
__device__ __forceinline__ float bfhi(u32 v) { return __builtin_bit_cast(float, v & 0xffff0000u); }
__device__ __forceinline__ float dot2(u32 a, u32 b, float c) {
  return __builtin_amdgcn_fdot2_f32_bf16(__builtin_bit_cast(bf16x2, a), __builtin_bit_cast(bf16x2, b), c, false);
}
template <int CTRL> __device__ __forceinline__ float dppf(float x) {
  return __builtin_bit_cast(float, __builtin_amdgcn_mov_dpp(__builtin_bit_cast(int, x), CTRL, 0xf, 0xf, true));
}
template <int CTRL> __device__ __forceinline__ u32 dppu(u32 x) {
  return (u32)__builtin_amdgcn_mov_dpp((int)x, CTRL, 0xf, 0xf, true);
}
constexpr int XOR1 = 0xB1, XOR2 = 0x4E, XOR7 = 0x141, XOR8 = 0x128, BCAST = 0x150;
__device__ __forceinline__ float xrow_max(float x) {
  auto s = __builtin_amdgcn_permlane16_swap(__float_as_uint(x), __float_as_uint(x), false, false);
  x = fmaxf(__uint_as_float(s[0]), __uint_as_float(s[1]));
  auto t = __builtin_amdgcn_permlane32_swap(__float_as_uint(x), __float_as_uint(x), false, false);
  return fmaxf(__uint_as_float(t[0]), __uint_as_float(t[1]));
}
__device__ __forceinline__ float xrow_sum(float x) {
  auto s = __builtin_amdgcn_permlane16_swap(__float_as_uint(x), __float_as_uint(x), false, false);
  x = __uint_as_float(s[0]) + __uint_as_float(s[1]);
  auto t = __builtin_amdgcn_permlane32_swap(__float_as_uint(x), __float_as_uint(x), false, false);
  return __uint_as_float(t[0]) + __uint_as_float(t[1]);
}
__device__ __forceinline__ float wave_max(float x) {
  x = fmaxf(x, dppf<XOR1>(x)); x = fmaxf(x, dppf<XOR2>(x)); x = fmaxf(x, dppf<XOR7>(x)); x = fmaxf(x, dppf<XOR8>(x));
  return xrow_max(x);
}
__device__ __forceinline__ float wave_sum(float x) {
  x += dppf<XOR1>(x); x += dppf<XOR2>(x); x += dppf<XOR7>(x); x += dppf<XOR8>(x);
  return xrow_sum(x);
}
__device__ __forceinline__ float xor40(float x) {
  const float y = dppf<XOR8>(x);
  auto t = __builtin_amdgcn_permlane32_swap(__float_as_uint(y), __float_as_uint(y), false, false);
  return (threadIdx.x & 32) ? __uint_as_float(t[0]) : __uint_as_float(t[1]);
}

typedef __amdgpu_buffer_rsrc_t rsrc_t;
__device__ __forceinline__ rsrc_t mkrsrc(const void* p, u32 bytes) {
  return __builtin_amdgcn_make_buffer_rsrc(const_cast<void*>(p), (short)0, (int)bytes, 0x00020000);
}
template <int AUX> __device__ __forceinline__ u32x4 bload(rsrc_t r, u32 voff, u32 soff) {
  return __builtin_amdgcn_raw_buffer_load_b128(r, (int)voff, (int)soff, AUX);
}

__device__ __forceinline__ f32x4 sel_mfma(u32x4 data, u32 coef, const u32 (&mk)[4], f32x4 acc) {
  u32x4 b = {coef & mk[0], coef & mk[1], coef & mk[2], coef & mk[3]};
  return __builtin_amdgcn_mfma_f32_16x16x32_bf16(__builtin_bit_cast(bf16x8, data), __builtin_bit_cast(bf16x8, b), acc, 0, 0, 0);
}
template <int T> __device__ __forceinline__ u32 bcast1(u32 v) { return dppu<BCAST + T>(v); }
template <int T> __device__ __forceinline__ u32 bcast2(u32 v) {
  const int a = __builtin_amdgcn_mov_dpp((int)v, BCAST + 2 * T, 0xf, 0xf, true);
  return (u32)__builtin_amdgcn_update_dpp(a, (int)v, BCAST + 2 * T + 1, 0xf, 0xc, false);
}

#define PIN(v) do { asm volatile("" : "+v"(v)); __builtin_amdgcn_sched_barrier(0); } while (0)
template <int I, int N, typename Fn> __device__ __forceinline__ void sfor(Fn&& f) {
  if constexpr (I < N) { f(std::integral_constant<int, I>{}); sfor<I + 1, N>(f); }
}

struct Args {
  const __bf16* x; const __bf16* kc; const __bf16* vc; const __bf16* wq; const __bf16* wo;
  const __bf16* w1; const __bf16* b1; const __bf16* w2; const __bf16* b2;
  float* part; __bf16* out;
};

__global__ void __launch_bounds__(NW * 64, 2) attn_mlp_main(Args a) {
  __shared__ __attribute__((aligned(16))) float lds[NW * 128 + 16 * 64 + NW * 128 + 2 * NW];
  float* qpart = lds;
  float* hpart = lds + NW * 128;
  float* opart = hpart + 16 * 64;
  float* mlw = opart + NW * 128;

  const int bid = blockIdx.x, h = bid & 31, b = bid >> 5;
  const int lane = threadIdx.x & 63, w = __builtin_amdgcn_readfirstlane(threadIdx.x >> 6);
  const int g = lane >> 4, c = lane & 15;
  const int nch = h < 12 ? 6 : 5;
  const int f0 = h < 12 ? 48 * h : 576 + 40 * (h - 12);

  u32 mk[4];
#pragma unroll
  for (int v = 0; v < 4; ++v) mk[v] = (((c & 7) >> 1) == v) ? ((c & 1) ? 0xffff0000u : 0x0000ffffu) : 0u;

  const rsrc_t rk = mkrsrc(a.kc + (size_t)bid * SK * DH, SK * DH * 2);
  const rsrc_t rv = mkrsrc(a.vc + (size_t)bid * SK * DH, SK * DH * 2);
  const rsrc_t rq = mkrsrc(a.wq, DM * NH * DH * 2);
  const rsrc_t ro = mkrsrc(a.wo, NH * DH * DM * 2);
  const rsrc_t r1 = mkrsrc(a.w1, DM * FF * 2);
  const rsrc_t r2 = mkrsrc(a.w2, FF * DM * 2);

  const u32x4 xr = *reinterpret_cast<const u32x4*>(a.x + b * DM + 8 * lane);
  const float xo = (float)a.x[b * DM + 64 * w + 4 * c + g];
  const float b1v = (float)a.b1[min(f0 + 4 * c + g, FF - 1)];

  u32x4 K[16], V[16];
  {
    const u32 vo = (u32)g * (NH * DH * 2) + c * 16, so = (u32)(64 * w) * (NH * DH * 2) + h * (DH * 2);
#pragma unroll
    for (int t = 0; t < 16; ++t) V[t] = bload<0>(rq, vo, so + t * 4 * (NH * DH * 2));
  }
  const u32 kvo = lane * 16;
#pragma unroll
  for (int t = 0; t < 16; ++t) K[t] = bload<KV_AUX>(rk, kvo + (t & 3) * 1024, w * 16384 + (t >> 2) * 4096);
  u32x4 W1[8];
  {
    const u32 vo = (u32)(4 * (c >> 3) + g) * (FF * 2) + min(c & 7, nch - 1) * 16, so = (u32)(64 * w) * (FF * 2) + f0 * 2;
#pragma unroll
    for (int t = 0; t < 8; ++t) W1[t] = bload<0>(r1, vo, so + t * 8 * (FF * 2));
  }
  __builtin_amdgcn_sched_barrier(0);

  float xv[8];
#pragma unroll
  for (int k = 0; k < 4; ++k) { xv[2 * k] = bflo(xr[k]); xv[2 * k + 1] = bfhi(xr[k]); }
  float s1 = 0.f;
#pragma unroll
  for (int k = 0; k < 8; ++k) s1 += xv[k];
  const float mean = wave_sum(s1) * (1.f / DM);
  float s2 = 0.f;
#pragma unroll
  for (int k = 0; k < 8; ++k) { const float d = xv[k] - mean; s2 = fmaf(d, d, s2); }
  const float var = bfr(wave_sum(s2) * (1.f / DM));
  const float den = bfr(sqrtf(bfr(var + 1e-5f)));
  const u32 xn = bfdup(bfr(xo - bfr(mean)) / den);

  {
    f32x4 acc = {0.f, 0.f, 0.f, 0.f};
    sfor<0, 16>([&](auto T) {
      constexpr int t = T;
      acc = sel_mfma(V[t], bcast1<t>(xn), mk, acc);
      PIN(acc);
      V[t] = bload<KV_AUX>(rv, kvo + (t & 3) * 1024, w * 16384 + (t >> 2) * 4096);
      __builtin_amdgcn_sched_barrier(0);
    });
#pragma unroll
    for (int r = 0; r < 4; ++r) qpart[w * 128 + 32 * g + 8 * r + (c & 7)] = acc[r];
  }
  __syncthreads();
  u32 q[4];
  {
    f32x4 lo = {0.f, 0.f, 0.f, 0.f}, hi = {0.f, 0.f, 0.f, 0.f};
#pragma unroll
    for (int u = 0; u < NW; ++u) {
      lo += *reinterpret_cast<const f32x4*>(qpart + u * 128 + 8 * c);
      hi += *reinterpret_cast<const f32x4*>(qpart + u * 128 + 8 * c + 4);
    }
    q[0] = bfbits(lo[0]) | (bfbits(lo[1]) << 16); q[1] = bfbits(lo[2]) | (bfbits(lo[3]) << 16);
    q[2] = bfbits(hi[0]) | (bfbits(hi[1]) << 16); q[3] = bfbits(hi[2]) | (bfbits(hi[3]) << 16);
  }
  __builtin_amdgcn_sched_barrier(0);

  float m = -INFINITY, l = 0.f;
  f32x4 oacc = {0.f, 0.f, 0.f, 0.f};
  u32x4 W2[6];

  auto tile = [&](auto MODE, int i) {
    constexpr int mode = MODE;
    const u32 nso = (u32)(w + NW * (i + 1)) * 16384;
    float x[16];
    sfor<0, 16>([&](auto T) {
      constexpr int t = T;
      float d = dot2(K[t][0], q[0], 0.f);
      d = dot2(K[t][1], q[1], d); d = dot2(K[t][2], q[2], d); d = dot2(K[t][3], q[3], d);
      PIN(d);
      x[t] = d;
      if constexpr (mode != 2) {
        K[t] = bload<KV_AUX>(rk, kvo + (t & 3) * 1024, nso + (t >> 2) * 4096);
      } else {
        K[t] = bload<0>(ro, (u32)(4 * (c >> 3) + g) * (DM * 2) + (c & 7) * 16, (u32)(h * DH + 8 * t) * (DM * 2) + w * 128);
      }
      __builtin_amdgcn_sched_barrier(0);
    });
    if constexpr (mode == 2) {
#pragma unroll
      for (int t = 0; t < 6; ++t)
        W2[t] = bload<0>(r2, (u32)(4 * (c >> 3) + g) * (DM * 2) + (c & 7) * 16, (u32)(f0 + 8 * t) * (DM * 2) + w * 128);
      __builtin_amdgcn_sched_barrier(0);
    }
    if constexpr (mode == 0) {
      f32x4 hacc = {0.f, 0.f, 0.f, 0.f};
      sfor<0, 8>([&](auto T) { constexpr int t = T; hacc = sel_mfma(W1[t], bcast2<t>(xn), mk, hacc); });
      float* hp = ((g >> 1) == (c >> 3)) ? hpart + (2 * w + (g >> 1)) * 64 : opart + w * 128 + (g >> 1) * 64;
#pragma unroll
      for (int r = 0; r < 4; ++r) hp[32 * (g & 1) + 8 * r + (c & 7)] = hacc[r];
      __builtin_amdgcn_sched_barrier(0);
    }
#define TR_STEP(HALF, CTRL, BIT)                                              \
  _Pragma("unroll") for (int j = 0; j < (HALF); ++j) {                        \
    const float t1 = x[j] + dppf<CTRL>(x[j]), t2 = x[j + (HALF)] + dppf<CTRL>(x[j + (HALF)]); \
    x[j] = (c & (BIT)) ? t2 : t1; }
    TR_STEP(8, XOR8, 8) TR_STEP(4, XOR7, 4) TR_STEP(2, XOR2, 2) TR_STEP(1, XOR1, 1)
#undef TR_STEP
    const float s = bfr(bfr(x[0]) * SCALE);
    const float mn = fmaxf(m, wave_max(s));
    const float alpha = __builtin_amdgcn_exp2f((m - mn) * LOG2E);
    const u32 pp = bfdup(__builtin_amdgcn_exp2f((s - mn) * LOG2E));
    l = fmaf(l, alpha, bflo(pp));
    m = mn;
    oacc *= alpha;
    __builtin_amdgcn_sched_barrier(0);
    sfor<0, 16>([&](auto T) {
      constexpr int t = T;
      oacc = sel_mfma(V[t], bcast1<t>(pp), mk, oacc);
      PIN(oacc);
      if constexpr (mode != 2) V[t] = bload<KV_AUX>(rv, kvo + (t & 3) * 1024, nso + (t >> 2) * 4096);
      __builtin_amdgcn_sched_barrier(0);
    });
  };

  tile(std::integral_constant<int, 0>{}, 0);
#pragma unroll 1
  for (int i = 1; i < NT - 1; ++i) tile(std::integral_constant<int, 1>{}, i);
  tile(std::integral_constant<int, 2>{}, NT - 1);

  l = wave_sum(l);
#pragma unroll
  for (int r = 0; r < 4; ++r) opart[w * 128 + 32 * g + 8 * r + (c & 7)] = oacc[r];
  if (lane == 0) { mlw[2 * w] = m; mlw[2 * w + 1] = l; }
  __syncthreads();
  u32 o0, o1, hh;
  {
    float M = -INFINITY;
#pragma unroll
    for (int u = 0; u < NW; ++u) M = fmaxf(M, mlw[2 * u]);
    float L = 0.f, a0 = 0.f, a1 = 0.f;
#pragma unroll
    for (int u = 0; u < NW; ++u) {
      const float wt = __builtin_amdgcn_exp2f((mlw[2 * u] - M) * LOG2E);
      L = fmaf(wt, mlw[2 * u + 1], L);
      a0 = fmaf(wt, opart[u * 128 + 4 * c + g], a0);
      a1 = fmaf(wt, opart[u * 128 + 64 + 4 * c + g], a1);
    }
    const float inv = 1.f / L;
    o0 = bfdup(a0 * inv); o1 = bfdup(a1 * inv);
    float hs = 0.f;
#pragma unroll
    for (int u = 0; u < 16; ++u) hs += hpart[u * 64 + 4 * c + g];
    float hv = bfr(bfr(hs) + b1v);
    hv = hv > 0.f ? hv : 0.f;
    hh = (4 * c + g < 8 * nch) ? bfdup(hv * hv) : 0u;
  }
  f32x4 aacc = {0.f, 0.f, 0.f, 0.f}, macc = {0.f, 0.f, 0.f, 0.f};
  sfor<0, 8>([&](auto T) { constexpr int t = T; aacc = sel_mfma(K[t], bcast2<t>(o0), mk, aacc); });
  sfor<0, 8>([&](auto T) { constexpr int t = T; aacc = sel_mfma(K[8 + t], bcast2<t>(o1), mk, aacc); });
  sfor<0, 6>([&](auto T) { constexpr int t = T; macc = sel_mfma(W2[t], bcast2<t>(hh), mk, macc); });
  f32x4 res;
#pragma unroll
  for (int r = 0; r < 4; ++r) {
    const float sa = aacc[r] + xor40(aacc[r]), sm = macc[r] + xor40(macc[r]);
    res[r] = (g < 2) ? sa : sm;
  }
  if ((g >> 1) == (c >> 3))
    *reinterpret_cast<f32x4*>(a.part + ((size_t)(b * NW + w) * NH + h) * 128 + (g * 8 + (c & 7)) * 4) = res;
}

__global__ void __launch_bounds__(64) final_reduce(Args a) {
  const int b = blockIdx.x >> 3, w = blockIdx.x & 7, lane = threadIdx.x, slot = lane & 31, hh = lane >> 5;
  const float* p = a.part + ((size_t)(b * NW + w) * NH + hh * 16) * 128 + slot * 4;
  f32x4 v[16];
#pragma unroll
  for (int k = 0; k < 16; ++k) v[k] = *reinterpret_cast<const f32x4*>(p + k * 128);
  const int col0 = 64 * w + 32 * ((slot >> 3) & 1) + (slot & 7);
  float xs[4], bs[4];
#pragma unroll
  for (int r = 0; r < 4; ++r) { xs[r] = (float)a.x[b * DM + col0 + 8 * r]; bs[r] = (float)a.b2[col0 + 8 * r]; }
  f32x4 s = v[0];
#pragma unroll
  for (int k = 1; k < 16; ++k) s += v[k];
#pragma unroll
  for (int r = 0; r < 4; ++r) {
    auto t = __builtin_amdgcn_permlane32_swap(__float_as_uint(s[r]), __float_as_uint(s[r]), false, false);
    const float tot = __uint_as_float(t[0]) + __uint_as_float(t[1]);
    auto u = __builtin_amdgcn_permlane16_swap(__float_as_uint(tot), __float_as_uint(tot), false, false);
    const float A = __uint_as_float(u[0]), Mv = __uint_as_float(u[1]);
    const float o = bfr(xs[r] + bfr(A)) + bfr(bfr(Mv) + bs[r]);
    if (lane < 16) a.out[b * DM + col0 + 8 * r] = (__bf16)o;
  }
}

extern "C" void kernel_launch(void* const* d_in, const int* in_sizes, int n_in, void* d_out, int out_size, void* d_ws,
                              size_t ws_size, hipStream_t stream) {
  Args a;
  a.x = (const __bf16*)d_in[0]; a.kc = (const __bf16*)d_in[1]; a.vc = (const __bf16*)d_in[2];
  a.wq = (const __bf16*)d_in[3]; a.wo = (const __bf16*)d_in[4];
  a.w1 = (const __bf16*)d_in[6]; a.b1 = (const __bf16*)d_in[7]; a.w2 = (const __bf16*)d_in[8]; a.b2 = (const __bf16*)d_in[9];
  a.part = (float*)d_ws; a.out = (__bf16*)d_out;
  attn_mlp_main<<<NB * NH, NW * 64, 0, stream>>>(a);
  final_reduce<<<NB * NW, 64, 0, stream>>>(a);
}
```

```cpp
#include <hip/hip_runtime.h>
#include <hip/hip_bf16.h>
#include <stdint.h>

typedef unsigned u32;
typedef u32 u32x4 __attribute__((ext_vector_type(4)));
typedef float f32x4 __attribute__((ext_vector_type(4)));
typedef __bf16 bf16x8 __attribute__((ext_vector_type(8)));
typedef __bf16 bf16x2 __attribute__((ext_vector_type(2)));

constexpr int NB = 8, DM = 512, NH = 32, SK = 4096, DH = 128, FF = 1376;
constexpr int NW = 8;
constexpr int NT = SK / 64 / NW;
constexpr float LOG2E = 1.4426950408889634f;
constexpr float SCALE = 0.08838834764831845f;

#define KV_AUX 2

__device__ __forceinline__ float bfr(float x) { return (float)(__bf16)x; }
__device__ __forceinline__ u32 bfbits(float x) { return (u32)__builtin_bit_cast(unsigned short, (__bf16)x); }
__device__ __forceinline__ u32 bfdup(float x) {
  bf16x2 v = {(__bf16)x, (__bf16)x};
  return __builtin_bit_cast(u32, v);
}
__device__ __forceinline__ float bflo(u32 v) { return __builtin_bit_cast(float, v << 16); }
__device__ __forceinline__ float bfhi(u32 v) { return __builtin_bit_cast(float, v & 0xffff0000u); }
__device__ __forceinline__ float dot2(u32 a, u32 b, float c) {
  return __builtin_amdgcn_fdot2_f32_bf16(__builtin_bit_cast(bf16x2, a), __builtin_bit_cast(bf16x2, b), c, false);
}
template <int CTRL> __device__ __forceinline__ float dppf(float x) {
  return __builtin_bit_cast(float, __builtin_amdgcn_mov_dpp(__builtin_bit_cast(int, x), CTRL, 0xf, 0xf, true));
}
template <int CTRL> __device__ __forceinline__ u32 dppu(u32 x) {
  return (u32)__builtin_amdgcn_mov_dpp((int)x, CTRL, 0xf, 0xf, true);
}
constexpr int XOR1 = 0xB1, XOR2 = 0x4E, XOR7 = 0x141, XOR8 = 0x128, BCAST = 0x150;
__device__ __forceinline__ float xrow_max(float x) {
  auto s = __builtin_amdgcn_permlane16_swap(__float_as_uint(x), __float_as_uint(x), false, false);
  x = fmaxf(__uint_as_float(s[0]), __uint_as_float(s[1]));
  auto t = __builtin_amdgcn_permlane32_swap(__float_as_uint(x), __float_as_uint(x), false, false);
  return fmaxf(__uint_as_float(t[0]), __uint_as_float(t[1]));
}
__device__ __forceinline__ float xrow_sum(float x) {
  auto s = __builtin_amdgcn_permlane16_swap(__float_as_uint(x), __float_as_uint(x), false, false);
  x = __uint_as_float(s[0]) + __uint_as_float(s[1]);
  auto t = __builtin_amdgcn_permlane32_swap(__float_as_uint(x), __float_as_uint(x), false, false);
  return __uint_as_float(t[0]) + __uint_as_float(t[1]);
}
__device__ __forceinline__ float wave_max(float x) {
  x = fmaxf(x, dppf<XOR1>(x)); x = fmaxf(x, dppf<XOR2>(x)); x = fmaxf(x, dppf<XOR7>(x)); x = fmaxf(x, dppf<XOR8>(x));
  return xrow_max(x);
}
__device__ __forceinline__ float wave_sum(float x) {
  x += dppf<XOR1>(x); x += dppf<XOR2>(x); x += dppf<XOR7>(x); x += dppf<XOR8>(x);
  return xrow_sum(x);
}
__device__ __forceinline__ float xor40(float x) {
  const float y = dppf<XOR8>(x);
  auto t = __builtin_amdgcn_permlane32_swap(__float_as_uint(y), __float_as_uint(y), false, false);
  return (threadIdx.x & 32) ? __uint_as_float(t[0]) : __uint_as_float(t[1]);
}

typedef __amdgpu_buffer_rsrc_t rsrc_t;
__device__ __forceinline__ rsrc_t mkrsrc(const void* p, u32 bytes) {
  return __builtin_amdgcn_make_buffer_rsrc(const_cast<void*>(p), (short)0, (int)bytes, 0x00020000);
}
template <int AUX> __device__ __forceinline__ u32x4 bload(rsrc_t r, u32 voff, u32 soff) {
  return __builtin_amdgcn_raw_buffer_load_b128(r, (int)voff, (int)soff, AUX);
}

__device__ __forceinline__ f32x4 sel_mfma(u32x4 data, u32 coef, const u32 (&mk)[4], f32x4 acc) {
  u32x4 b = {coef & mk[0], coef & mk[1], coef & mk[2], coef & mk[3]};
  return __builtin_amdgcn_mfma_f32_16x16x32_bf16(__builtin_bit_cast(bf16x8, data), __builtin_bit_cast(bf16x8, b), acc, 0, 0, 0);
}
template <int T> __device__ __forceinline__ u32 bcast1(u32 v) { return dppu<BCAST + T>(v); }
template <int T> __device__ __forceinline__ u32 bcast2(u32 v) {
  const int a = __builtin_amdgcn_mov_dpp((int)v, BCAST + 2 * T, 0xf, 0xf, true);
  return (u32)__builtin_amdgcn_update_dpp(a, (int)v, BCAST + 2 * T + 1, 0xf, 0xc, false);
}

#define PIN(v) do { asm volatile("" : "+v"(v)); __builtin_amdgcn_sched_barrier(0); } while (0)
template <int I, int N, typename Fn> __device__ __forceinline__ void sfor(Fn&& f) {
  if constexpr (I < N) { f(std::integral_constant<int, I>{}); sfor<I + 1, N>(f); }
}

#define PST(k)
#define STAMP(k)
#define STAMP_DECL
#define STAMP_OUT(slot)
struct Args {
  const __bf16* x; const __bf16* kc; const __bf16* vc; const __bf16* wq; const __bf16* wo;
  const __bf16* w1; const __bf16* b1; const __bf16* w2; const __bf16* b2;
  float* part; __bf16* out;
};

#define RD 3
constexpr int RS = RD <= 2 ? 2 : RD <= 4 ? 4 : RD <= 8 ? 8 : 16;
#define T0N 8
#define MIX 0
#define NBURST 8
#define W_AUX 2
#define WQ_AUX W_AUX
#define ABL_WQ 1
#define ABL_W 1
#define ROTQ 1
#define SB() __builtin_amdgcn_sched_barrier(0)
#define LAS __attribute__((address_space(3)))
typedef short s16x4 __attribute__((ext_vector_type(4)));
typedef u32 u32x2 __attribute__((ext_vector_type(2)));
typedef float f32x2 __attribute__((ext_vector_type(2)));
template <int V> using IC = std::integral_constant<int, V>;

struct PSeq { int code[16 + 32]; int pos[16]; };
constexpr PSeq make_pseq() {
  PSeq p{}; int n = 0, k = 0;
  for (int t = 0; t < 16; ++t) {
    while (k < ((t + 1) * MIX + 15) / 16) p.code[n++] = 16 + k++;
    p.pos[t] = n; p.code[n++] = t;
  }
  while (k < T0N) p.code[n++] = 16 + k++;
  return p;
}
constexpr PSeq PS = make_pseq();
constexpr int PLEN = 16 + T0N;
constexpr int imin(int a, int b) { return a < b ? a : b; }
static_assert(MIX <= T0N && RD <= T0N && T0N <= 32, "");

constexpr int L_WO = 0;
constexpr int L_PART = L_WO + NW * 16384;
constexpr int L_HP = L_PART + NW * 512;
constexpr int L_DUMP = L_HP + 16 * 256;
constexpr int L_VEC = L_DUMP + NW * 512;
constexpr int L_ML = L_VEC + NW * 256;
constexpr int L_END = L_ML + NW * 8;

__global__ void __launch_bounds__(NW * 64, 2) attn_mlp_main(Args a) {
  __shared__ __attribute__((aligned(16))) char lds[L_END];
  STAMP_DECL; STAMP(0);
  const int bid = blockIdx.x, h = bid & 31, b = bid >> 5;
  const int lane = threadIdx.x & 63, w = __builtin_amdgcn_readfirstlane(threadIdx.x >> 6);
  const int g = lane >> 4, c = lane & 15;

  const rsrc_t rk = mkrsrc(a.kc + (size_t)bid * SK * DH, SK * DH * 2);
  const rsrc_t rv = mkrsrc(a.vc + (size_t)bid * SK * DH, SK * DH * 2);
  const rsrc_t rq = mkrsrc(a.wq, DM * NH * DH * 2 * ABL_WQ);
  const u32 kvo = lane * 16 + w * 1024;
  auto sload = [&](auto J, u32 so) {
    constexpr int j = J;
    return bload<KV_AUX>(j < 16 ? rk : rv, kvo, so + (j & 15) * (NW * 1024));
  };

  const int wr = ROTQ ? (w + b) & 7 : w, tr = ROTQ ? 2 * b : 0;
  const u32x4 xr = __builtin_nontemporal_load(reinterpret_cast<const u32x4*>(a.x + b * DM + 8 * lane));
  const float xo = (float)__builtin_nontemporal_load(a.x + b * DM + 64 * wr + 4 * ((c + tr) & 15) + g);
  u32x4 T0[32], WQ[16];
  const u32 qvo = (u32)g * (NH * DH * 2) + c * 16, qso = (u32)(64 * wr) * (NH * DH * 2) + h * (DH * 2);
  auto pissue = [&](auto N) {
    constexpr int code = PS.code[N];
    if constexpr (code < 16) WQ[code] = bload<WQ_AUX>(rq, qvo, qso + ((code + tr) & 15) * 4 * (NH * DH * 2));
    else T0[code - 16] = sload(IC<code - 16>{}, 0u);
  };
  sfor<0, imin(NBURST, PLEN)>(pissue);
  SB(); PST(1);

  u32 mk[4];
#pragma unroll
  for (int v = 0; v < 4; ++v) mk[v] = (((c & 7) >> 1) == v) ? ((c & 1) ? 0xffff0000u : 0x0000ffffu) : 0u;

  u32 xn;
  {
    float xv[8];
#pragma unroll
    for (int k = 0; k < 4; ++k) { xv[2 * k] = bflo(xr[k]); xv[2 * k + 1] = bfhi(xr[k]); }
    const float s1 = ((xv[0] + xv[1]) + (xv[2] + xv[3])) + ((xv[4] + xv[5]) + (xv[6] + xv[7]));
    const float mean = wave_sum(s1) * (1.f / DM);
    float s2 = 0.f, s3 = 0.f;
#pragma unroll
    for (int k = 0; k < 4; ++k) { const float d = xv[k] - mean, e = xv[k + 4] - mean; s2 = fmaf(d, d, s2); s3 = fmaf(e, e, s3); }
    const float var = bfr(wave_sum(s2 + s3) * (1.f / DM));
    const float den = bfr(__builtin_amdgcn_sqrtf(bfr(var + 1e-5f)));
    xn = bfdup(bfr(xo - bfr(mean)) * __builtin_amdgcn_rcpf(den));
  }
  PIN(xn); PST(2);
  STAMP(1);
  float* part = reinterpret_cast<float*>(lds + L_PART);
  {
    f32x4 acc = {0.f, 0.f, 0.f, 0.f};
    sfor<0, 16>([&](auto T) {
      constexpr int t = T;
      acc = sel_mfma(WQ[t], bcast1<t>(xn), mk, acc);
      PIN(acc);
      constexpr int lo = imin(PLEN, t == 0 ? NBURST : PS.pos[t - 1] + 1 + NBURST), hi = imin(PLEN, PS.pos[t] + 1 + NBURST);
      if constexpr (t < 15) sfor<lo, hi>(pissue);
      SB();
    });
    PST(3);
#pragma unroll
    for (int r = 0; r < 4; ++r) part[w * 128 + 32 * g + 8 * r + (c & 7)] = acc[r];
    SB();
    sfor<imin(PLEN, PS.pos[14] + 1 + NBURST), PLEN>(pissue);
    SB();
  }
  PST(4);
  __syncthreads();
  PST(5);
  u32* vec = reinterpret_cast<u32*>(lds + L_VEC) + w * 64;
  u32x4 q;
  {
    f32x2 s = {0.f, 0.f};
#pragma unroll
    for (int u = 0; u < NW; ++u) s += *reinterpret_cast<const f32x2*>(part + u * 128 + 2 * lane);
    vec[lane] = bfbits(s[0]) | (bfbits(s[1]) << 16);
    __builtin_amdgcn_wave_barrier();
    q = *reinterpret_cast<const u32x4*>(vec + 4 * c);
  }
  PIN(q); PST(6);
  STAMP(2);

  const int nch = h < 12 ? 6 : 5;
  const int f0 = h < 12 ? 48 * h : 576 + 40 * (h - 12);
  const rsrc_t ro = mkrsrc(a.wo, NH * DH * DM * 2 * ABL_W);
  const rsrc_t r1 = mkrsrc(a.w1, DM * FF * 2 * ABL_W);
  const rsrc_t r2 = mkrsrc(a.w2, FF * DM * 2 * ABL_W);
  float m = -INFINITY, l = 0.f, b1v;
  f32x4 oacc = {0.f, 0.f, 0.f, 0.f}, hacc = {0.f, 0.f, 0.f, 0.f};
  u32x4 R[RS], WO[16], W2[6], W1[8];
  u32 hh;
  float* hpart = reinterpret_cast<float*>(lds + L_HP);
  char* wol = lds + L_WO + w * 16384;

  auto side = [&](auto MODE, auto J) {
    constexpr int mode = MODE, j = J;
    if constexpr (mode == 0) {
      if constexpr (j < 8) {
        W1[j] = bload<W_AUX>(r1, (u32)(4 * (c >> 3) + g) * (FF * 2) + min(c & 7, nch - 1) * 16, (u32)(64 * wr + 4 * ((2 * j + tr) & 15)) * (FF * 2) + f0 * 2);
      } else if constexpr (j < 24) {
        WO[j - 8] = bload<W_AUX>(ro, (u32)(lane >> 3) * (DM * 2) + (lane & 7) * 16, (u32)(h * DH + 8 * (j - 8)) * (DM * 2) + w * 128);
      } else if constexpr (j < 30) {
        W2[j - 24] = bload<W_AUX>(r2, (u32)(4 * (c >> 3) + g) * (DM * 2) + (c & 7) * 16, (u32)(f0 + 8 * (j - 24)) * (DM * 2) + w * 128);
      } else if constexpr (j == 30) {
        b1v = (float)__builtin_nontemporal_load(a.b1 + min(f0 + 4 * c + g, FF - 1));
      }
    } else if constexpr (mode == 1) {
      if constexpr (j < 8) { hacc = sel_mfma(W1[j], bcast2<j>(xn), mk, hacc); asm volatile("" : "+v"(hacc)); }
      if constexpr (j < 16) *reinterpret_cast<u32x4*>(wol + j * 1024 + lane * 16) = WO[j];
      if constexpr (j == 8) {
        float* hp = ((g >> 1) == (c >> 3)) ? hpart + (2 * w + (g >> 1)) * 64 : reinterpret_cast<float*>(lds + L_DUMP) + w * 128 + (g >> 1) * 64;
#pragma unroll
        for (int r = 0; r < 4; ++r) hp[32 * (g & 1) + 8 * r + (c & 7)] = hacc[r];
      }
      if constexpr (j == 12) __syncthreads();
      if constexpr (j == 13) {
        float hs = 0.f;
#pragma unroll
        for (int u = 0; u < 16; ++u) hs += hpart[u * 64 + 4 * c + g];
        float hv = bfr(bfr(hs) + b1v);
        hv = hv > 0.f ? hv : 0.f;
        hh = (4 * c + g < 8 * nch) ? bfdup(hv * hv) : 0u;
        asm volatile("" : "+v"(hh));
        hacc = f32x4{0.f, 0.f, 0.f, 0.f};
      }
      if constexpr (j >= 14 && j < 20) { hacc = sel_mfma(W2[j - 14], bcast2<j - 14>(hh), mk, hacc); asm volatile("" : "+v"(hacc)); }
      if constexpr (j == 20) {
        float* pm = a.part + (size_t)(NB * NH + bid) * DM + 64 * w + 32 * (g & 1) + (c & 7);
        const bool st = g < 2 && c < 8;
#pragma unroll
        for (int r = 0; r < 4; ++r) { const float v = hacc[r] + xor40(hacc[r]); if (st) pm[8 * r] = v; }
      }
      if constexpr (j >= 16) {
        constexpr int t = j - 16;
        const char* p = wol + (32 * (t & 3) + 8 * g + (c >> 2)) * 128 + (t >> 2) * 32 + (c & 3) * 8;
        const s16x4 lo = __builtin_amdgcn_ds_read_tr16_b64_v4i16((LAS s16x4*)p), hi = __builtin_amdgcn_ds_read_tr16_b64_v4i16((LAS s16x4*)(p + 512));
        const u32x2 l2 = __builtin_bit_cast(u32x2, lo), h2 = __builtin_bit_cast(u32x2, hi);
        WO[t] = u32x4{l2[0], l2[1], h2[0], h2[1]};
      }
    }
  };

  auto tile = [&](auto MODE, u32 cso, u32 nso) {
    constexpr int mode = MODE;
    auto issue = [&](auto J) {
      constexpr int j = J, n = j + RD;
      if constexpr (n >= 32) R[n % RS] = sload(IC<n - 32>{}, nso);
      else if constexpr (mode != 0) R[n % RS] = sload(IC<n>{}, cso);
      else if constexpr (n >= T0N) T0[n] = sload(IC<n>{}, cso);
      SB();
      side(MODE, J);
      SB();
    };
    float x[16];
    sfor<0, 16>([&](auto T) {
      constexpr int t = T;
      const u32x4 k = mode == 0 ? T0[t] : R[t % RS];
      float d = dot2(k[0], q[0], 0.f);
      d = dot2(k[1], q[1], d); d = dot2(k[2], q[2], d); d = dot2(k[3], q[3], d);
      PIN(d);
      x[t] = d;
      issue(T);
    });
#define TR_STEP(HALF, CTRL, BIT)                                              \
  _Pragma("unroll") for (int j = 0; j < (HALF); ++j) {                        \
    const float t1 = x[j] + dppf<CTRL>(x[j]), t2 = x[j + (HALF)] + dppf<CTRL>(x[j + (HALF)]); \
    x[j] = (c & (BIT)) ? t2 : t1; }
    TR_STEP(8, XOR8, 8) TR_STEP(4, XOR7, 4) TR_STEP(2, XOR2, 2) TR_STEP(1, XOR1, 1)
#undef TR_STEP
    const float s = bfr(bfr(x[0]) * SCALE);
    const float mn = fmaxf(m, wave_max(s));
    const float alpha = __builtin_amdgcn_exp2f((m - mn) * LOG2E);
    const u32 pp = bfdup(__builtin_amdgcn_exp2f((s - mn) * LOG2E));
    m = mn;
    oacc *= alpha;
    SB();
    sfor<0, 16>([&](auto T) {
      constexpr int t = T;
      oacc = sel_mfma(mode == 0 ? T0[16 + t] : R[(16 + t) % RS], bcast1<t>(pp), mk, oacc);
      PIN(oacc);
      issue(IC<16 + t>{});
      if constexpr (t == 2) { l = fmaf(l, alpha, wave_sum(bflo(pp))); PIN(l); }
    });
  };

  constexpr u32 STB = NW * 16384;
  tile(IC<0>{}, 0u, STB);
  tile(IC<1>{}, STB, 2 * STB);
#pragma unroll 1
  for (u32 so = 2 * STB; so < SK * DH * 2; so += STB) tile(IC<2>{}, so, so + STB);

  STAMP(3);
#pragma unroll
  for (int r = 0; r < 4; ++r) part[w * 128 + 32 * g + 8 * r + (c & 7)] = oacc[r];
  *reinterpret_cast<f32x2*>(lds + L_ML + w * 8) = f32x2{m, l};
  __syncthreads();
  STAMP(4);
  {
    f32x4 ml[NW / 2]; f32x2 op[NW];
#pragma unroll
    for (int u = 0; u < NW / 2; ++u) ml[u] = *reinterpret_cast<const f32x4*>(lds + L_ML + u * 16);
#pragma unroll
    for (int u = 0; u < NW; ++u) op[u] = *reinterpret_cast<const f32x2*>(part + u * 128 + 2 * lane);
    SB();
    float M = -INFINITY;
#pragma unroll
    for (int u = 0; u < NW; ++u) M = fmaxf(M, ml[u / 2][2 * (u & 1)]);
    float L = 0.f; f32x2 o = {0.f, 0.f};
#pragma unroll
    for (int u = 0; u < NW; ++u) {
      const float wt = __builtin_amdgcn_exp2f((ml[u / 2][2 * (u & 1)] - M) * LOG2E);
      L = fmaf(wt, ml[u / 2][2 * (u & 1) + 1], L);
      o += wt * op[u];
    }
    o *= __builtin_amdgcn_rcpf(L);
    vec[lane] = bfbits(o[0]) | (bfbits(o[1]) << 16);
    __builtin_amdgcn_wave_barrier();
  }
  u32x4 ob[4];
#pragma unroll
  for (int ks = 0; ks < 4; ++ks) ob[ks] = *reinterpret_cast<const u32x4*>(vec + 16 * ks + 4 * g);
  f32x4 acc[4];
#pragma unroll
  for (int nt = 0; nt < 4; ++nt) acc[nt] = f32x4{0.f, 0.f, 0.f, 0.f};
#pragma unroll
  for (int ks = 0; ks < 4; ++ks)
#pragma unroll
    for (int nt = 0; nt < 4; ++nt)
      acc[nt] = __builtin_amdgcn_mfma_f32_16x16x32_bf16(__builtin_bit_cast(bf16x8, WO[4 * nt + ks]), __builtin_bit_cast(bf16x8, ob[ks]), acc[nt], 0, 0, 0);
  const f32x4 r01 = (c & 1) ? acc[1] : acc[0], r23 = (c & 1) ? acc[3] : acc[2], res = (c & 2) ? r23 : r01;
  if (c < 4) *reinterpret_cast<f32x4*>(a.part + (size_t)bid * DM + 64 * w + 16 * c + 4 * g) = res;
  STAMP(5); STAMP_OUT(bid * NW + w);
}

__global__ void __launch_bounds__(64, 1) final_reduce(Args a) {
  STAMP_DECL; STAMP(6);
  const int b = blockIdx.x >> 3, w = blockIdx.x & 7, lane = threadIdx.x, g = lane >> 4, c = lane & 15;
  const rsrc_t rp = mkrsrc(a.part, 2 * NB * NH * DM * 4);
  const u32 vo = (u32)g * (8 * DM * 4) + c * 16, so = (u32)b * (NH * DM * 4) + w * 256;
  f32x4 va[8], vm[8];
#pragma unroll
  for (int k = 0; k < 8; ++k) {
    va[k] = __builtin_bit_cast(f32x4, bload<0>(rp, vo + (k & 1) * (DM * 4), so + (k >> 1) * (2 * DM * 4)));
    vm[k] = __builtin_bit_cast(f32x4, bload<0>(rp, vo + (k & 1) * (DM * 4), so + (k >> 1) * (2 * DM * 4) + NB * NH * DM * 4));
  }
  const int col = 64 * w + 4 * c + 2 * (g & 1);
  u32 xs = *reinterpret_cast<const u32*>(a.x + b * DM + col), bs = *reinterpret_cast<const u32*>(a.b2 + col);
  SB();
  const f32x4 sa = ((va[0] + va[1]) + (va[2] + va[3])) + ((va[4] + va[5]) + (va[6] + va[7]));
  const f32x4 sm = ((vm[0] + vm[1]) + (vm[2] + vm[3])) + ((vm[4] + vm[5]) + (vm[6] + vm[7]));
  float y[4];
#pragma unroll
  for (int r = 0; r < 4; ++r) {
    auto t = __builtin_amdgcn_permlane32_swap(__float_as_uint(sa[r]), __float_as_uint(sm[r]), false, false);
    y[r] = __uint_as_float(t[0]) + __uint_as_float(t[1]);
  }
  float o[2];
  asm volatile("" : "+v"(xs), "+v"(bs));
#pragma unroll
  for (int r = 0; r < 2; ++r) {
    auto t = __builtin_amdgcn_permlane16_swap(__float_as_uint(y[r]), __float_as_uint(y[r + 2]), false, false);
    const float z = __uint_as_float(t[0]) + __uint_as_float(t[1]);
    auto u = __builtin_amdgcn_permlane32_swap(__float_as_uint(z), __float_as_uint(z), false, false);
    const float A = __uint_as_float(u[0]), Mv = __uint_as_float(u[1]);
    o[r] = bfr((r ? bfhi(xs) : bflo(xs)) + bfr(A)) + bfr(bfr(Mv) + (r ? bfhi(bs) : bflo(bs)));
  }
  if (lane < 32) *reinterpret_cast<u32*>(a.out + b * DM + col) = bfbits(o[0]) | (bfbits(o[1]) << 16);
  STAMP(7); STAMP_OUT(2048 + blockIdx.x);
}

extern "C" void kernel_launch(void* const* d_in, const int* in_sizes, int n_in, void* d_out, int out_size, void* d_ws,
                              size_t ws_size, hipStream_t stream) {
  Args a;
  a.x = (const __bf16*)d_in[0]; a.kc = (const __bf16*)d_in[1]; a.vc = (const __bf16*)d_in[2];
  a.wq = (const __bf16*)d_in[3]; a.wo = (const __bf16*)d_in[4];
  a.w1 = (const __bf16*)d_in[6]; a.b1 = (const __bf16*)d_in[7]; a.w2 = (const __bf16*)d_in[8]; a.b2 = (const __bf16*)d_in[9];
  a.part = (float*)d_ws; a.out = (__bf16*)d_out;
  attn_mlp_main<<<NB * NH, NW * 64, 0, stream>>>(a);
  final_reduce<<<NB * NW, 64, 0, stream>>>(a);
}
```

```cpp
#include <hip/hip_runtime.h>
#include <hip/hip_bf16.h>
#include <stdint.h>
#include <type_traits>

typedef unsigned u32;
typedef u32 u32x2 __attribute__((ext_vector_type(2)));
typedef u32 u32x4 __attribute__((ext_vector_type(4)));
typedef float f32x2 __attribute__((ext_vector_type(2)));
typedef float f32x4 __attribute__((ext_vector_type(4)));
typedef short s16x4 __attribute__((ext_vector_type(4)));
typedef __bf16 bf16x2 __attribute__((ext_vector_type(2)));
typedef __bf16 bf16x8 __attribute__((ext_vector_type(8)));
typedef __amdgpu_buffer_rsrc_t rsrc_t;
#define LAS __attribute__((address_space(3)))
#define RLX_AGENT __ATOMIC_RELAXED, __HIP_MEMORY_SCOPE_AGENT

constexpr int NB = 8, DM = 512, NH = 32, SK = 4096, DH = 128, FF = 1376;
constexpr int NW = 8, NTHR = NW * 64 + 64;
constexpr int NT = SK / 64 / NW;
constexpr u32 STB = NW * 16384, KVB = SK * DH * 2;
constexpr float LOG2E = 1.4426950408889634f, SCALE = 0.08838834764831845f;
constexpr int AUX_NT = 2, AUX_SC1 = 16;
constexpr u32 OOB = 0x80000000u;
constexpr u32 SENT = 0xffffffffu;

#define RD 4
#define PW 5
#define KLEAD 2
#define KMIX 25
#define LNPOS 6
#define AC1 6320
#define AC2 3360
#define PWAIT 24
#define PABS 50
constexpr float KW = 1.022f;
constexpr int RS = 4;
static_assert(RD <= RS, "");

#define SB() __builtin_amdgcn_sched_barrier(0)
#define PIN(v) do { asm volatile("" : "+v"(v)); SB(); } while (0)
template <int V> using IC = std::integral_constant<int, V>;
template <int I, int N, typename Fn> __device__ __forceinline__ void sfor(Fn&& f) {
  if constexpr (I < N) { f(IC<I>{}); sfor<I + 1, N>(f); }
}
__device__ __forceinline__ float bfr(float x) { return (float)(__bf16)x; }
__device__ __forceinline__ u32 bfpack(float lo, float hi) { bf16x2 v = {(__bf16)lo, (__bf16)hi}; return __builtin_bit_cast(u32, v); }
__device__ __forceinline__ float bflo(u32 v) { return __builtin_bit_cast(float, v << 16); }
__device__ __forceinline__ float bfhi(u32 v) { return __builtin_bit_cast(float, v & 0xffff0000u); }
__device__ __forceinline__ float dot2(u32 a, u32 b, float c) {
  return __builtin_amdgcn_fdot2_f32_bf16(__builtin_bit_cast(bf16x2, a), __builtin_bit_cast(bf16x2, b), c, false);
}
template <int CTRL> __device__ __forceinline__ u32 dppu(u32 x) { return (u32)__builtin_amdgcn_mov_dpp((int)x, CTRL, 0xf, 0xf, true); }
template <int CTRL> __device__ __forceinline__ float dppf(float x) { return __uint_as_float(dppu<CTRL>(__float_as_uint(x))); }
constexpr int XOR1 = 0xB1, XOR2 = 0x4E, XOR7 = 0x141, ROR = 0x120, XOR8 = ROR + 8, BCAST = 0x150;
__device__ __forceinline__ f32x2 swap16(float a, float b) {
  auto s = __builtin_amdgcn_permlane16_swap(__float_as_uint(a), __float_as_uint(b), false, false);
  return f32x2{__uint_as_float(s[0]), __uint_as_float(s[1])};
}
__device__ __forceinline__ f32x2 swap32(float a, float b) {
  auto s = __builtin_amdgcn_permlane32_swap(__float_as_uint(a), __float_as_uint(b), false, false);
  return f32x2{__uint_as_float(s[0]), __uint_as_float(s[1])};
}
__device__ __forceinline__ float wave_max(float x) {
  x = fmaxf(x, dppf<XOR1>(x)); x = fmaxf(x, dppf<XOR2>(x)); x = fmaxf(x, dppf<XOR7>(x)); x = fmaxf(x, dppf<XOR8>(x));
  f32x2 s = swap16(x, x); x = fmaxf(s[0], s[1]);
  s = swap32(x, x); return fmaxf(s[0], s[1]);
}
__device__ __forceinline__ float wave_sum(float x) {
  x += dppf<XOR1>(x); x += dppf<XOR2>(x); x += dppf<XOR7>(x); x += dppf<XOR8>(x);
  f32x2 s = swap16(x, x); x = s[0] + s[1];
  s = swap32(x, x); return s[0] + s[1];
}
__device__ __forceinline__ u32 clock32() { return (u32)__builtin_amdgcn_s_memrealtime(); }
__device__ __forceinline__ rsrc_t mkrsrc(const void* p, u32 bytes) {
  return __builtin_amdgcn_make_buffer_rsrc(const_cast<void*>(p), (short)0, (int)bytes, 0x00020000);
}
template <int AUX> __device__ __forceinline__ u32x4 bload(rsrc_t r, u32 voff, u32 soff) {
  return __builtin_amdgcn_raw_buffer_load_b128(r, (int)voff, (int)soff, AUX);
}
template <int AUX> __device__ __forceinline__ void bstore(u32x4 v, rsrc_t r, u32 voff, u32 soff) {
  __builtin_amdgcn_raw_buffer_store_b128(v, r, (int)voff, (int)soff, AUX);
}
__device__ __forceinline__ f32x4 mfma(u32x4 a, u32x4 b, f32x4 c) {
  return __builtin_amdgcn_mfma_f32_16x16x32_bf16(__builtin_bit_cast(bf16x8, a), __builtin_bit_cast(bf16x8, b), c, 0, 0, 0);
}
__device__ __forceinline__ f32x4 sel_mfma(u32x4 data, u32 coef, const u32 (&mk)[4], f32x4 acc) {
  return mfma(data, u32x4{coef & mk[0], coef & mk[1], coef & mk[2], coef & mk[3]}, acc);
}
template <int T> __device__ __forceinline__ u32 bcast1(u32 v) { return dppu<BCAST + T>(v); }
template <int T> __device__ __forceinline__ u32 bcast2(u32 v) {
  const int a = __builtin_amdgcn_mov_dpp((int)v, BCAST + 2 * T, 0xf, 0xf, true);
  return (u32)__builtin_amdgcn_update_dpp(a, (int)v, BCAST + 2 * T + 1, 0xf, 0xc, false);
}
constexpr int vmcnt_imm(int n) { return (n & 15) | ((n >> 4) << 14) | 0x0f70; }

struct PSeq { int code[64]; };
constexpr PSeq make_pseq() {
  PSeq p{}; int n = 0, k = 0;
  for (int t = 0; t < 16; ++t) {
    while (k < KLEAD + (t * (KMIX - KLEAD)) / 15) p.code[n++] = 16 + k++;
    p.code[n++] = t;
  }
  while (n < 64) p.code[n++] = 16 + k++;
  return p;
}
constexpr PSeq PS = make_pseq();
constexpr int PLAST = 16 + KMIX;
constexpr int KA = KMIX + PW + 1;
constexpr int KB = KA + 4;
static_assert(KB <= 32 + RD && KB >= 16 + RD && KLEAD <= PW + 1 && LNPOS >= KLEAD, "");

constexpr int L_PART = 0;
constexpr int L_HP = L_PART + NW * 512;
constexpr int L_TMP = L_HP + 16 * 256;
constexpr int L_VEC = L_TMP + NW * 512;
constexpr int L_ML = L_VEC + NW * 256;
constexpr int L_FLAG = L_ML + NW * 8;
constexpr int L_WO = L_FLAG + 64;
constexpr int L_END = L_WO + NW * 16384;
static_assert(L_WO % 16 == 0 && L_END <= 160 * 1024, "");

__global__ void __launch_bounds__(NTHR, 3)
fused_decode_block(const __bf16* kc, const __bf16* wq, const __bf16* x, const __bf16* vc, float* ws, const __bf16* wo, const __bf16* w1,
                   const __bf16* w2, const __bf16* b1, const __bf16* b2, __bf16* out) {
  __shared__ __attribute__((aligned(16))) char lds[L_END];
  asm volatile("" :: "s"(kc), "s"(wq), "s"(x), "s"(vc), "s"(ws), "s"(wo), "s"(w1), "s"(w2));
  const int bid = blockIdx.x, h = bid & 31, b = bid >> 5;
  const int lane = threadIdx.x & 63, w = __builtin_amdgcn_readfirstlane(threadIdx.x >> 6);
  const int g = lane >> 4, c = lane & 15;
  const rsrc_t rp = mkrsrc(ws + (size_t)b * (NH * DM), NH * DM * 4);
  volatile LAS u32* flag = (volatile LAS u32*)(lds + L_FLAG);

  if (w == NW) {
    __builtin_amdgcn_s_setprio(3);
    const u32 cso = (u32)h * 64;
    u32 o0 = (u32)(lane >> 2) * 2048 + (lane & 3) * 16, o1 = o0 + 32768;
    u32* tok = reinterpret_cast<u32*>(ws) + NB * NH * DM + b * 64 + lane;
    __builtin_amdgcn_s_sleep(64);
    const u32 snap = __hip_atomic_load(tok, RLX_AGENT);
    bstore<AUX_SC1>(u32x4{SENT, SENT, SENT, SENT}, rp, o0, cso);
    bstore<AUX_SC1>(u32x4{SENT, SENT, SENT, SENT}, rp, o1, cso);
    const float xb = (float)x[b * DM + 16 * h + 4 * (c & 3) + g], bb = (float)b2[16 * h + 4 * (c & 3) + g];
    flag[1] = 0u;
    asm volatile("s_waitcnt vmcnt(0)" ::: "memory");
    u32 v;
    auto meantime = [&](int ln, u32 t) -> u32 {
      if (lane == 32 * ln + h) __hip_atomic_store(tok, t == snap ? t + 1 : t, RLX_AGENT);
      __builtin_amdgcn_s_sleep(127);
      v = __hip_atomic_load(tok, RLX_AGENT);
      const bool ok = v != snap && (lane >> 5) == ln;
      const int n = max(__builtin_popcountll(__builtin_amdgcn_ballot_w64(ok)), 1);
      return t + (int)(wave_sum(ok ? (float)min(max((int)(v - t), -2000), 2000) : 0.f) * __builtin_amdgcn_rcpf((float)n));
    };
    __syncthreads();
    flag[1] = (meantime(0, clock32()) + AC1) | 1u;
    __syncthreads();
    const u32 te = meantime(1, clock32()) + AC2;
    flag[1] = te | 1u;
    const bool rep = (u32)__builtin_amdgcn_ballot_w64(v == snap) != 0;
    flag[0] = rep ? 1u : 0u;
    __syncthreads();
    u32x4 v0 = {0u, 0u, 0u, 0u}, v1 = {0u, 0u, 0u, 0u};
    __builtin_amdgcn_s_sleep(PWAIT);
    while ((int)(clock32() - (te + PABS)) < 0) __builtin_amdgcn_s_sleep(1);
#pragma unroll 1
    for (int spin = 0; spin < (1 << 18); ++spin) {
      asm volatile("" ::: "memory");
      const u32x4 n0 = bload<AUX_SC1>(rp, o0, cso), n1 = bload<AUX_SC1>(rp, o1, cso);
      const bool m0 = max(max(n0[0], n0[1]), max(n0[2], n0[3])) == SENT, m1 = max(max(n1[0], n1[1]), max(n1[2], n1[3])) == SENT;
      if (!m0) { v0 |= n0; o0 = OOB; }
      if (!m1) { v1 |= n1; o1 = OOB; }
      const int miss = __builtin_popcountll(__builtin_amdgcn_ballot_w64(m0)) + __builtin_popcountll(__builtin_amdgcn_ballot_w64(m1));
      if (miss == 0) break;
      if (miss > 32) __builtin_amdgcn_s_sleep(6); else __builtin_amdgcn_s_sleep(2);
    }
    if (o0 != OOB || o1 != OOB) v0 = u32x4{SENT, SENT, SENT, SENT};
    const f32x4 s = __builtin_bit_cast(f32x4, v0) + __builtin_bit_cast(f32x4, v1);
    const f32x2 t0 = swap32(s[0], s[2]), t1 = swap32(s[1], s[3]);
    const f32x2 t2 = swap16(t0[0] + t0[1], t1[0] + t1[1]);
    float z = t2[0] + t2[1];
    z += dppf<ROR + 4>(z); z += dppf<XOR8>(z);
    if (c < 4) out[b * DM + 16 * h + 4 * c + g] = (__bf16)((xb + z) + bb);
    if (rep) __syncthreads();
    return;
  }

  const __bf16* kb = kc + (size_t)bid * SK * DH;
  const __bf16* vb = vc + (size_t)bid * SK * DH;
  const rsrc_t rk = mkrsrc(kb, KVB), rv = mkrsrc(vb, KVB), rq = mkrsrc(wq, DM * NH * DH * 2);
  const u32 kvo = lane * 16 + w * 1024;
  auto sload = [&](rsrc_t r, int t, u32 so) { return bload<AUX_NT>(r, kvo, so + t * (NW * 1024)); };

  const int wr = (w + b) & 7, tr = 2 * b;
  u32x4 T0[32], WQ[16], R[RS];
  auto kissue = [&](auto N) { constexpr int n = N; if constexpr (n < 32) T0[n] = sload(rk, n, 0u); else R[(n - 32) % RS] = sload(rk, n - 32, 2 * STB); };
  const u32 qvo = (u32)g * (NH * DH * 2) + c * 16, qso = (u32)(64 * wr) * (NH * DH * 2) + h * (DH * 2);
  auto pissue = [&](auto N) {
    constexpr int code = PS.code[N];
    if constexpr (code < 16) WQ[code] = bload<0>(rq, qvo, qso + ((code + tr) & 15) * 4 * (NH * DH * 2));
    else kissue(IC<code - 16>{});
  };
  sfor<0, KLEAD>(pissue);
  SB();
  const u32x4 xr = *reinterpret_cast<const u32x4*>(x + b * DM + 8 * lane);
  const float xo = (float)x[b * DM + 64 * wr + 4 * ((c + tr) & 15) + g];
  SB();
  sfor<KLEAD, PW + 1>(pissue);
  SB();
  sfor<0, LNPOS>([&](auto N) {
    constexpr int n = N;
    __builtin_amdgcn_s_waitcnt(vmcnt_imm(n < KLEAD ? PW + 2 : PW)); SB();
    pissue(IC<n + PW + 1>{});
    SB();
  });

  u32 mk[4];
#pragma unroll
  for (int v = 0; v < 4; ++v) mk[v] = (((c & 7) >> 1) == v) ? ((c & 1) ? 0xffff0000u : 0x0000ffffu) : 0u;
  u32 xn;
  {
    float xv[8];
#pragma unroll
    for (int k = 0; k < 4; ++k) { xv[2 * k] = bflo(xr[k]); xv[2 * k + 1] = bfhi(xr[k]); }
    const float s1 = ((xv[0] + xv[1]) + (xv[2] + xv[3])) + ((xv[4] + xv[5]) + (xv[6] + xv[7]));
    const float mean = wave_sum(s1) * (1.f / DM);
    float s2 = 0.f, s3 = 0.f;
#pragma unroll
    for (int k = 0; k < 4; ++k) { const float d = xv[k] - mean, e = xv[k + 4] - mean; s2 = fmaf(d, d, s2); s3 = fmaf(e, e, s3); }
    const float var = bfr(wave_sum(s2 + s3) * (1.f / DM));
    const float den = bfr(__builtin_amdgcn_sqrtf(bfr(var + 1e-5f)));
    const float v = bfr(xo - bfr(mean)) * __builtin_amdgcn_rcpf(den);
    xn = bfpack(v, v);
  }
  PIN(xn);
  float* part = reinterpret_cast<float*>(lds + L_PART);
  float* pw = part + w * 128 + 32 * g + (c & 7);
  {
    f32x4 acc = {0.f, 0.f, 0.f, 0.f};
    sfor<0, PLAST>([&](auto N) {
      constexpr int n = N, code = PS.code[n];
      if constexpr (code < 16) { acc = sel_mfma(WQ[code], bcast1<code>(xn), mk, acc); PIN(acc); }
      else if constexpr (n >= LNPOS) { __builtin_amdgcn_s_waitcnt(vmcnt_imm(PW)); SB(); }
      if constexpr (n >= LNPOS) pissue(IC<n + PW + 1>{});
      SB();
    });
#pragma unroll
    for (int r = 0; r < 4; ++r) pw[8 * r] = acc[r];
    SB();
    kissue(IC<KA>{});
    SB();
  }
  __syncthreads();
  u32* vec = reinterpret_cast<u32*>(lds + L_VEC) + w * 64;
  auto sum_parts = [&]() {
    f32x2 p[NW];
#pragma unroll
    for (int u = 0; u < NW; ++u) p[u] = *reinterpret_cast<const f32x2*>(part + u * 128 + 2 * lane);
    const f32x2 s = ((p[0] + p[1]) + (p[2] + p[3])) + ((p[4] + p[5]) + (p[6] + p[7]));
    vec[lane] = bfpack(s[0], s[1]);
    __builtin_amdgcn_wave_barrier();
  };
  sum_parts();
  u32x4 q = *reinterpret_cast<const u32x4*>(vec + 4 * c);
  PIN(q);
  kissue(IC<KA + 1>{});
  SB();

  const int sl = (h & 7) * 4 + (h >> 3);
  const int nch = sl < 12 ? 6 : 5;
  const int f0 = sl < 12 ? 48 * sl : 576 + 40 * (sl - 12);
  const rsrc_t ro = mkrsrc(wo, NH * DH * DM * 2), r1 = mkrsrc(w1, DM * FF * 2), r2 = mkrsrc(w2, FF * DM * 2);
  char* wol = lds + L_WO + w * 16384;
  const u32 w1vo = (u32)(4 * (c >> 3) + g) * (FF * 2) + min(c & 7, nch - 1) * 16;
  const u32 wovo = (u32)(lane >> 3) * (DM * 2) + (lane & 7) * 16;
  const u32 wost = (u32)(lane >> 3) * 128 + (((lane & 7) ^ (((lane >> 4) & 1) << 1)) * 16);
  f32x4 oacc = {0.f, 0.f, 0.f, 0.f}, hacc = {0.f, 0.f, 0.f, 0.f};
  float sc[8];

  u32 due, pdt;
  auto repace = [&](float rem, float unit) {
    const u32 te = flag[1], now = clock32();
    if ((int)((now << 10) - due) > 0) due = now << 10;
    const float left = te ? fmaxf((float)(int)(te - now), 0.f) : 0.f;
    pdt = __builtin_amdgcn_readfirstlane((u32)(left * (2048.f * unit) * __builtin_amdgcn_rcpf(rem)));
  };
#define PACE_PRE(t) u32 now_; if constexpr ((t) % 2 == 0) now_ = clock32();
#define PACE_POST(t) if constexpr ((t) % 2 == 0) { while ((int)((now_ << 10) - due) < 0) now_ = clock32(); due += pdt; SB(); }

  auto ktile = [&](auto MODE, u32 i) {
    constexpr int mode = MODE;
    const u32 so = i * STB;
    const bool last = i == NT - 1;
    const rsrc_t rn = mkrsrc(last ? vb : kb, KVB);
    const u32 sn = last ? 0u : so + STB;
    if constexpr (mode == 3) repace(128.f + KW * 16.f * (float)(NT - i), KW);
    u32x4 sw[9];
    auto sld = [&](auto J, u32 gi) {
      constexpr int j = J;
      if constexpr (j % 3 == 0) sw[j] = bload<0>(r1, w1vo, (u32)(64 * wr + 4 * ((2 * gi + tr) & 15)) * (FF * 2) + f0 * 2);
      else sw[j] = bload<0>(ro, wovo, (u32)(h * DH + 16 * gi + 8 * (j % 3 - 1)) * (DM * 2) + w * 128);
    };
    auto suse = [&](auto J, u32 gi) {
      constexpr int j = J;
      if constexpr (j % 3 == 0) {
        hacc = sel_mfma(sw[j], bcast2<0>(xn), mk, hacc); asm volatile("" : "+v"(hacc));
        xn = dppu<ROR + 14>(xn);
      } else *reinterpret_cast<u32x4*>(wol + gi * 2048 + (j % 3 - 1) * 1024 + (wost ^ ((j % 3 - 1) * 64))) = sw[j];
    };
    float s[16];
    sfor<0, 16>([&](auto T) {
      constexpr int t = T, n = t + RD;
      PACE_PRE(mode == 3 ? t : 1)
      const u32x4 k = mode < 2 ? T0[(16 * mode + t) & 31] : R[t % RS];
      float d = dot2(k[0], q[0], 0.f);
      d = dot2(k[1], q[1], d); d = dot2(k[2], q[2], d); d = dot2(k[3], q[3], d);
      PIN(d);
      s[t] = d;
      PACE_POST(mode == 3 ? t : 1)
      if constexpr (mode < 2) {
        constexpr int j = 16 * mode + t;
        if constexpr (j == 8) kissue(IC<KA + 2>{});
        if constexpr (j == 15) kissue(IC<KA + 3>{});
        if constexpr (j + RD >= KB) kissue(IC<j + RD>{});
      } else {
        if constexpr (n >= 16) R[n % RS] = sload(rn, n - 16, sn); else R[n % RS] = sload(rk, n, so);
      }
      SB();
      if constexpr (mode == 3) {
        if constexpr (t % 5 == 0 && t < 15) sld(IC<t / 5>{}, i);
        if constexpr (t % 5 == 0 && t > 0) suse(IC<t / 5 - 1>{}, i);
      }
      if constexpr (mode == 2) {
        if constexpr (t < 9) sld(T, t / 3);
        if constexpr (t >= 7) suse(IC<t - 7>{}, (t - 7) / 3);
      }
      SB();
    });
#define TR_STEP(HALF, CTRL, BIT)                                                                \
  _Pragma("unroll") for (int j = 0; j < (HALF); ++j) {                                          \
    const float t1 = s[j] + dppf<CTRL>(s[j]), t2 = s[j + (HALF)] + dppf<CTRL>(s[j + (HALF)]);   \
    s[j] = (c & (BIT)) ? t2 : t1; }
    TR_STEP(8, XOR8, 8) TR_STEP(4, XOR7, 4) TR_STEP(2, XOR2, 2) TR_STEP(1, XOR1, 1)
#undef TR_STEP
#pragma unroll
    for (int k = 7; k > 0; --k) sc[k] = sc[k - 1];
    sc[0] = bfr(bfr(s[0]) * SCALE);
    PIN(sc[0]);
  };

  u32 pp[8], hh, svo;
  float cw, b1v;
  u32x4 WO[16], W2[6];
  f32x4 acc[4];
  float* hpart = reinterpret_cast<float*>(lds + L_HP);
  float* tmp = reinterpret_cast<float*>(lds + L_TMP) + w * 128;
  const bool diag = (g >> 1) == (c >> 3);
  auto vside = [&](auto J) {
    constexpr int j = J;
    if constexpr (j == 0) b1v = (float)b1[min(f0 + 4 * c + g, FF - 1)];
    if constexpr (j >= 1 && j < 7)
      W2[j - 1] = bload<0>(r2, (u32)(4 * (c >> 3) + g) * (DM * 2) + (c & 7) * 16, (u32)(f0 + 8 * (j - 1)) * (DM * 2) + w * 128);
    if constexpr (j == 4) __syncthreads();
    if constexpr (j == 5) {
      float hs = 0.f;
#pragma unroll
      for (int u = 0; u < 16; ++u) hs += hpart[u * 64 + 4 * c + g];
      float hv = bfr(bfr(hs) + b1v);
      hv = hv > 0.f ? hv : 0.f;
      hh = (4 * c + g < 8 * nch) ? bfpack(hv * hv, hv * hv) : 0u;
      asm volatile("" : "+v"(hh));
      hacc = f32x4{0.f, 0.f, 0.f, 0.f};
    }
    if constexpr (j == 6) {
      f32x4 ml[NW / 2];
#pragma unroll
      for (int u = 0; u < NW / 2; ++u) ml[u] = *reinterpret_cast<const f32x4*>(lds + L_ML + u * 16);
      float M = -INFINITY, L = 0.f;
#pragma unroll
      for (int u = 0; u < NW; ++u) M = fmaxf(M, ml[u / 2][2 * (u & 1)]);
#pragma unroll
      for (int u = 0; u < NW; ++u) L = fmaf(__builtin_amdgcn_exp2f((ml[u / 2][2 * (u & 1)] - M) * LOG2E), ml[u / 2][2 * (u & 1) + 1], L);
      cw = __builtin_amdgcn_exp2f((cw - M) * LOG2E) * __builtin_amdgcn_rcpf(L);
      asm volatile("" : "+v"(cw));
    }
    if constexpr (j == 7) {
      svo = c < 4 ? (u32)c * 64 + g * 16 : OOB;
      asm volatile("" : "+v"(svo));
    }
    if constexpr (j >= 8 && j < 14) { hacc = sel_mfma(W2[j - 8], bcast2<j - 8>(hh), mk, hacc); asm volatile("" : "+v"(hacc)); }
    if constexpr (j == 14) {
#pragma unroll
      for (int r = 0; r < 4; ++r) {
        const float y = dppf<XOR8>(hacc[r]);
        const f32x2 t = swap32(y, y);
        tmp[(diag ? 0 : 64) + 32 * (g & 1) + 8 * r + (c & 7)] = hacc[r] + ((lane & 32) ? t[0] : t[1]);
      }
      __builtin_amdgcn_wave_barrier();
    }
    if constexpr (j == 15) {
#pragma unroll
      for (int nt = 0; nt < 4; ++nt) acc[nt] = *reinterpret_cast<const f32x4*>(tmp + 16 * nt + 4 * g);
    }
    {
      const int row = 8 * g + (c >> 2), swz = ((row >> 1) & 1) | (((row >> 3) & 1) << 1);
      const char* p = wol + (32 * (j & 3) + row) * 128 + (((j >> 2) ^ swz) * 32) + (c & 3) * 8;
      const u32x2 lo = __builtin_bit_cast(u32x2, __builtin_amdgcn_ds_read_tr16_b64_v4i16((LAS s16x4*)p));
      const u32x2 hi = __builtin_bit_cast(u32x2, __builtin_amdgcn_ds_read_tr16_b64_v4i16((LAS s16x4*)(p + 512)));
      WO[j] = u32x4{lo[0], lo[1], hi[0], hi[1]};
    }
  };
  auto vtile = [&](auto FIRST, u32 so) {
    repace((float)(KVB - so) * (16.f / STB), 1.f);
    const u32 p = pp[7];
#pragma unroll
    for (int k = 7; k > 0; --k) pp[k] = pp[k - 1];
    sfor<0, 16>([&](auto T) {
      constexpr int t = T, n = t + RD;
      PACE_PRE(t)
      oacc = sel_mfma(R[t % RS], bcast1<t>(p), mk, oacc);
      PIN(oacc);
      if (n < 16 || so + STB < KVB) { PACE_POST(t) }
      R[n % RS] = sload(rv, n, so);
      SB();
      if constexpr (FIRST) { vside(T); SB(); }
    });
  };

#pragma unroll
  for (int k = 0; k < 8; ++k) sc[k] = 0.f;
  ktile(IC<0>{}, 0u);
  ktile(IC<1>{}, 1u);
  ktile(IC<2>{}, 2u);
  due = clock32() << 10;
#pragma unroll 1
  for (u32 i = 3; i < NT; ++i) ktile(IC<3>{}, i);
  {
    float* hp = diag ? hpart + (2 * w + (g >> 1)) * 64 : tmp + (g >> 1) * 64;
#pragma unroll
    for (int r = 0; r < 4; ++r) hp[32 * (g & 1) + 8 * r + (c & 7)] = hacc[r];
    float m = fmaxf(fmaxf(fmaxf(sc[0], sc[1]), fmaxf(sc[2], sc[3])), fmaxf(fmaxf(sc[4], sc[5]), fmaxf(sc[6], sc[7])));
    m = wave_max(m);
    float l = 0.f;
#pragma unroll
    for (int k = 0; k < 8; ++k) { const float e = __builtin_amdgcn_exp2f((sc[k] - m) * LOG2E); pp[k] = bfpack(e, e); l += bflo(pp[k]); }
    l = wave_sum(l);
    *reinterpret_cast<f32x2*>(lds + L_ML + w * 8) = f32x2{m, l};
    cw = m;
    SB();
  }
  vtile(IC<1>{}, 0u);
#pragma unroll 1
  for (u32 so = STB; so < KVB; so += STB) vtile(IC<0>{}, so);

#pragma unroll
  for (int r = 0; r < 4; ++r) pw[8 * r] = oacc[r] * cw;
  __syncthreads();
  sum_parts();
  u32x4 ob[4];
#pragma unroll
  for (int ks = 0; ks < 4; ++ks) ob[ks] = *reinterpret_cast<const u32x4*>(vec + 16 * ks + 4 * g);
  SB();
#pragma unroll
  for (int ks = 0; ks < 4; ++ks)
#pragma unroll
    for (int nt = 0; nt < 4; ++nt) acc[nt] = mfma(WO[4 * nt + ks], ob[ks], acc[nt]);
  const f32x4 r01 = (c & 1) ? acc[1] : acc[0], r23 = (c & 1) ? acc[3] : acc[2];
  u32x4 ru = __builtin_bit_cast(u32x4, (c & 2) ? r23 : r01);
#pragma unroll
  for (int r = 0; r < 4; ++r) ru[r] = min(ru[r], SENT - 1);
  const u32 sso = (u32)h * 2048 + w * 256;
  bstore<AUX_SC1>(ru, rp, svo, sso);
  SB();
#pragma unroll
  for (int k = 0; k < RS; ++k) asm volatile("" :: "v"(R[k]));
  if (__builtin_amdgcn_readfirstlane(flag[0]) != 0) {
    __syncthreads();
    bstore<AUX_SC1>(ru, rp, svo, sso);
  }
}

extern "C" void kernel_launch(void* const* d_in, const int* in_sizes, int n_in, void* d_out, int out_size, void* d_ws,
                              size_t ws_size, hipStream_t stream) {
  fused_decode_block<<<NB * NH, NTHR, 0, stream>>>(
      (const __bf16*)d_in[1], (const __bf16*)d_in[3], (const __bf16*)d_in[0], (const __bf16*)d_in[2], (float*)d_ws,
      (const __bf16*)d_in[4], (const __bf16*)d_in[6], (const __bf16*)d_in[8], (const __bf16*)d_in[7], (const __bf16*)d_in[9],
      (__bf16*)d_out);
}
```

```cpp
#include <hip/hip_runtime.h>
#include <hip/hip_bf16.h>
#include <stdint.h>
#include <type_traits>

typedef unsigned u32;
typedef u32 u32x2 __attribute__((ext_vector_type(2)));
typedef u32 u32x4 __attribute__((ext_vector_type(4)));
typedef float f32x2 __attribute__((ext_vector_type(2)));
typedef float f32x4 __attribute__((ext_vector_type(4)));
typedef short s16x4 __attribute__((ext_vector_type(4)));
typedef __bf16 bf16x2 __attribute__((ext_vector_type(2)));
typedef __bf16 bf16x8 __attribute__((ext_vector_type(8)));
typedef __amdgpu_buffer_rsrc_t rsrc_t;
#define LAS __attribute__((address_space(3)))
#define RLX_AGENT __ATOMIC_RELAXED, __HIP_MEMORY_SCOPE_AGENT

constexpr int NB = 8, DM = 512, NH = 32, SK = 4096, DH = 128, FF = 1376;
constexpr int NW = 8, NTHR = NW * 64 + 64;
constexpr int NT = SK / 64 / NW;
constexpr u32 STB = NW * 16384, KVB = SK * DH * 2;
constexpr float LOG2E = 1.4426950408889634f, SCALE = 0.08838834764831845f;
constexpr int AUX_NT = 2, AUX_SC1 = 16;
constexpr u32 OOB = 0x80000000u;
constexpr u32 SENT = 0xffffffffu;

#define RD 4
#define PW 5
#define KLEAD 2
#define KMIX 25
#define LNPOS 6
#define AC1 6400
#define AC2 3370
#define PWAIT 24
#define PABS 50
constexpr float KW = 1.02f;
constexpr int RS = 4;
static_assert(RD <= RS, "");

#define SB() __builtin_amdgcn_sched_barrier(0)
#define PIN(v) do { asm volatile("" : "+v"(v)); SB(); } while (0)
template <int V> using IC = std::integral_constant<int, V>;
template <int I, int N, typename Fn> __device__ __forceinline__ void sfor(Fn&& f) {
  if constexpr (I < N) { f(IC<I>{}); sfor<I + 1, N>(f); }
}
__device__ __forceinline__ float bfr(float x) { return (float)(__bf16)x; }
__device__ __forceinline__ u32 bfpack(float lo, float hi) { bf16x2 v = {(__bf16)lo, (__bf16)hi}; return __builtin_bit_cast(u32, v); }
__device__ __forceinline__ float bflo(u32 v) { return __builtin_bit_cast(float, v << 16); }
__device__ __forceinline__ float bfhi(u32 v) { return __builtin_bit_cast(float, v & 0xffff0000u); }
__device__ __forceinline__ float dot2(u32 a, u32 b, float c) {
  return __builtin_amdgcn_fdot2_f32_bf16(__builtin_bit_cast(bf16x2, a), __builtin_bit_cast(bf16x2, b), c, false);
}
template <int CTRL> __device__ __forceinline__ u32 dppu(u32 x) { return (u32)__builtin_amdgcn_mov_dpp((int)x, CTRL, 0xf, 0xf, true); }
template <int CTRL> __device__ __forceinline__ float dppf(float x) { return __uint_as_float(dppu<CTRL>(__float_as_uint(x))); }
constexpr int XOR1 = 0xB1, XOR2 = 0x4E, XOR7 = 0x141, ROR = 0x120, XOR8 = ROR + 8, BCAST = 0x150;
__device__ __forceinline__ f32x2 swap16(float a, float b) {
  auto s = __builtin_amdgcn_permlane16_swap(__float_as_uint(a), __float_as_uint(b), false, false);
  return f32x2{__uint_as_float(s[0]), __uint_as_float(s[1])};
}
__device__ __forceinline__ f32x2 swap32(float a, float b) {
  auto s = __builtin_amdgcn_permlane32_swap(__float_as_uint(a), __float_as_uint(b), false, false);
  return f32x2{__uint_as_float(s[0]), __uint_as_float(s[1])};
}
__device__ __forceinline__ float wave_max(float x) {
  x = fmaxf(x, dppf<XOR1>(x)); x = fmaxf(x, dppf<XOR2>(x)); x = fmaxf(x, dppf<XOR7>(x)); x = fmaxf(x, dppf<XOR8>(x));
  f32x2 s = swap16(x, x); x = fmaxf(s[0], s[1]);
  s = swap32(x, x); return fmaxf(s[0], s[1]);
}
__device__ __forceinline__ float wave_sum(float x) {
  x += dppf<XOR1>(x); x += dppf<XOR2>(x); x += dppf<XOR7>(x); x += dppf<XOR8>(x);
  f32x2 s = swap16(x, x); x = s[0] + s[1];
  s = swap32(x, x); return s[0] + s[1];
}
__device__ __forceinline__ u32 clock32() { return (u32)__builtin_amdgcn_s_memrealtime(); }
__device__ __forceinline__ rsrc_t mkrsrc(const void* p, u32 bytes) {
  return __builtin_amdgcn_make_buffer_rsrc(const_cast<void*>(p), (short)0, (int)bytes, 0x00020000);
}
template <int AUX> __device__ __forceinline__ u32x4 bload(rsrc_t r, u32 voff, u32 soff) {
  return __builtin_amdgcn_raw_buffer_load_b128(r, (int)voff, (int)soff, AUX);
}
template <int AUX> __device__ __forceinline__ void bstore(u32x4 v, rsrc_t r, u32 voff, u32 soff) {
  __builtin_amdgcn_raw_buffer_store_b128(v, r, (int)voff, (int)soff, AUX);
}
__device__ __forceinline__ f32x4 mfma(u32x4 a, u32x4 b, f32x4 c) {
  return __builtin_amdgcn_mfma_f32_16x16x32_bf16(__builtin_bit_cast(bf16x8, a), __builtin_bit_cast(bf16x8, b), c, 0, 0, 0);
}
__device__ __forceinline__ f32x4 sel_mfma(u32x4 data, u32 coef, const u32 (&mk)[4], f32x4 acc) {
  return mfma(data, u32x4{coef & mk[0], coef & mk[1], coef & mk[2], coef & mk[3]}, acc);
}
template <int T> __device__ __forceinline__ u32 bcast1(u32 v) { return dppu<BCAST + T>(v); }
template <int T> __device__ __forceinline__ u32 bcast2(u32 v) {
  const int a = __builtin_amdgcn_mov_dpp((int)v, BCAST + 2 * T, 0xf, 0xf, true);
  return (u32)__builtin_amdgcn_update_dpp(a, (int)v, BCAST + 2 * T + 1, 0xf, 0xc, false);
}
constexpr int vmcnt_imm(int n) { return (n & 15) | ((n >> 4) << 14) | 0x0f70; }

struct PSeq { int code[64]; };
constexpr PSeq make_pseq() {
  PSeq p{}; int n = 0, k = 0;
  for (int t = 0; t < 16; ++t) {
    while (k < KLEAD + (t * (KMIX - KLEAD)) / 15) p.code[n++] = 16 + k++;
    p.code[n++] = t;
  }
  while (n < 64) p.code[n++] = 16 + k++;
  return p;
}
constexpr PSeq PS = make_pseq();
constexpr int PLAST = 16 + KMIX;
constexpr int KA = KMIX + PW + 1;
constexpr int KB = KA + 4;
static_assert(KB <= 32 + RD && KB >= 16 + RD && KLEAD <= PW + 1 && LNPOS >= KLEAD, "");

constexpr int L_PART = 0;
constexpr int L_HP = L_PART + NW * 512;
constexpr int L_TMP = L_HP + 16 * 256;
constexpr int L_VEC = L_TMP + NW * 512;
constexpr int L_ML = L_VEC + NW * 256;
constexpr int L_FLAG = L_ML + NW * 8;
constexpr int L_WO = L_FLAG + 64;
constexpr int L_END = L_WO + NW * 16384;
static_assert(L_WO % 16 == 0 && L_END <= 160 * 1024, "");

__global__ void __launch_bounds__(NTHR, 3)
fused_decode_block(const __bf16* kc, const __bf16* wq, const __bf16* x, const __bf16* vc, float* ws, const __bf16* wo, const __bf16* w1,
                   const __bf16* w2, const __bf16* b1, const __bf16* b2, __bf16* out) {
  __shared__ __attribute__((aligned(16))) char lds[L_END];
  asm volatile("" :: "s"(kc), "s"(wq), "s"(x), "s"(vc), "s"(ws), "s"(wo), "s"(w1), "s"(w2));
  const int bid = blockIdx.x, h = bid & 31, b = bid >> 5;
  const int lane = threadIdx.x & 63, w = __builtin_amdgcn_readfirstlane(threadIdx.x >> 6);
  const int g = lane >> 4, c = lane & 15;
  const rsrc_t rp = mkrsrc(ws + (size_t)b * (NH * DM), NH * DM * 4);
  volatile LAS u32* flag = (volatile LAS u32*)(lds + L_FLAG);

  if (w == NW) {
    __builtin_amdgcn_s_setprio(3);
    const u32 cso = (u32)h * 64;
    u32 o0 = (u32)(lane >> 2) * 2048 + (lane & 3) * 16, o1 = o0 + 32768;
    u32* tok = reinterpret_cast<u32*>(ws) + NB * NH * DM + b * 64 + lane;
    __builtin_amdgcn_s_sleep(64);
    const u32 snap = __hip_atomic_load(tok, RLX_AGENT);
    bstore<AUX_SC1>(u32x4{SENT, SENT, SENT, SENT}, rp, o0, cso);
    bstore<AUX_SC1>(u32x4{SENT, SENT, SENT, SENT}, rp, o1, cso);
    const float xb = (float)x[b * DM + 16 * h + 4 * (c & 3) + g], bb = (float)b2[16 * h + 4 * (c & 3) + g];
    flag[1] = 0u;
    asm volatile("s_waitcnt vmcnt(0)" ::: "memory");
    u32 v;
    auto meantime = [&](int ln, u32 t) -> u32 {
      if (lane == 32 * ln + h) __hip_atomic_store(tok, t == snap ? t + 1 : t, RLX_AGENT);
      __builtin_amdgcn_s_sleep(127);
      v = __hip_atomic_load(tok, RLX_AGENT);
      const bool ok = v != snap && (lane >> 5) == ln;
      const int n = max(__builtin_popcountll(__builtin_amdgcn_ballot_w64(ok)), 1);
      return t + (int)(wave_sum(ok ? (float)min(max((int)(v - t), -2000), 2000) : 0.f) * __builtin_amdgcn_rcpf((float)n));
    };
    __syncthreads();
    flag[1] = (meantime(0, clock32()) + AC1) | 1u;
    __syncthreads();
    const u32 te = meantime(1, clock32()) + AC2;
    flag[1] = te | 1u;
    const bool rep = (u32)__builtin_amdgcn_ballot_w64(v == snap) != 0;
    flag[0] = rep ? 1u : 0u;
    __syncthreads();
    u32x4 v0 = {0u, 0u, 0u, 0u}, v1 = {0u, 0u, 0u, 0u};
    __builtin_amdgcn_s_sleep(PWAIT);
    while ((int)(clock32() - (te + PABS)) < 0) __builtin_amdgcn_s_sleep(1);
#pragma unroll 1
    for (int spin = 0; spin < (1 << 18); ++spin) {
      asm volatile("" ::: "memory");
      const u32x4 n0 = bload<AUX_SC1>(rp, o0, cso), n1 = bload<AUX_SC1>(rp, o1, cso);
      const bool m0 = max(max(n0[0], n0[1]), max(n0[2], n0[3])) == SENT, m1 = max(max(n1[0], n1[1]), max(n1[2], n1[3])) == SENT;
      if (!m0) { v0 |= n0; o0 = OOB; }
      if (!m1) { v1 |= n1; o1 = OOB; }
      const int miss = __builtin_popcountll(__builtin_amdgcn_ballot_w64(m0)) + __builtin_popcountll(__builtin_amdgcn_ballot_w64(m1));
      if (miss == 0) break;
      if (miss > 32) __builtin_amdgcn_s_sleep(6); else __builtin_amdgcn_s_sleep(2);
    }
    if (o0 != OOB || o1 != OOB) v0 = u32x4{SENT, SENT, SENT, SENT};
    const f32x4 s = __builtin_bit_cast(f32x4, v0) + __builtin_bit_cast(f32x4, v1);
    const f32x2 t0 = swap32(s[0], s[2]), t1 = swap32(s[1], s[3]);
    const f32x2 t2 = swap16(t0[0] + t0[1], t1[0] + t1[1]);
    float z = t2[0] + t2[1];
    z += dppf<ROR + 4>(z); z += dppf<XOR8>(z);
    if (c < 4) out[b * DM + 16 * h + 4 * c + g] = (__bf16)((xb + z) + bb);
    if (rep) __syncthreads();
    return;
  }

  const __bf16* kb = kc + (size_t)bid * SK * DH;
  const __bf16* vb = vc + (size_t)bid * SK * DH;
  const rsrc_t rk = mkrsrc(kb, KVB), rv = mkrsrc(vb, KVB), rq = mkrsrc(wq, DM * NH * DH * 2);
  const u32 kvo = lane * 16 + w * 1024;
  auto sload = [&](rsrc_t r, int t, u32 so) { return bload<AUX_NT>(r, kvo, so + t * (NW * 1024)); };

  const int wr = (w + b) & 7, tr = 2 * b;
  u32x4 T0[32], WQ[16], R[RS];
  auto kissue = [&](auto N) { constexpr int n = N; if constexpr (n < 32) T0[n] = sload(rk, n, 0u); else R[(n - 32) % RS] = sload(rk, n - 32, 2 * STB); };
  const u32 qvo = (u32)g * (NH * DH * 2) + c * 16, qso = (u32)(64 * wr) * (NH * DH * 2) + h * (DH * 2);
  auto pissue = [&](auto N) {
    constexpr int code = PS.code[N];
    if constexpr (code < 16) WQ[code] = bload<0>(rq, qvo, qso + ((code + tr) & 15) * 4 * (NH * DH * 2));
    else kissue(IC<code - 16>{});
  };
  sfor<0, KLEAD>(pissue);
  SB();
  const u32x4 xr = *reinterpret_cast<const u32x4*>(x + b * DM + 8 * lane);
  const float xo = (float)x[b * DM + 64 * wr + 4 * ((c + tr) & 15) + g];
  SB();
  sfor<KLEAD, PW + 1>(pissue);
  SB();
  sfor<0, LNPOS>([&](auto N) {
    constexpr int n = N;
    __builtin_amdgcn_s_waitcnt(vmcnt_imm(n < KLEAD ? PW + 2 : PW)); SB();
    pissue(IC<n + PW + 1>{});
    SB();
  });

  u32 mk[4];
#pragma unroll
  for (int v = 0; v < 4; ++v) mk[v] = (((c & 7) >> 1) == v) ? ((c & 1) ? 0xffff0000u : 0x0000ffffu) : 0u;
  u32 xn;
  {
    float xv[8];
#pragma unroll
    for (int k = 0; k < 4; ++k) { xv[2 * k] = bflo(xr[k]); xv[2 * k + 1] = bfhi(xr[k]); }
    const float s1 = ((xv[0] + xv[1]) + (xv[2] + xv[3])) + ((xv[4] + xv[5]) + (xv[6] + xv[7]));
    const float mean = wave_sum(s1) * (1.f / DM);
    float s2 = 0.f, s3 = 0.f;
#pragma unroll
    for (int k = 0; k < 4; ++k) { const float d = xv[k] - mean, e = xv[k + 4] - mean; s2 = fmaf(d, d, s2); s3 = fmaf(e, e, s3); }
    const float var = bfr(wave_sum(s2 + s3) * (1.f / DM));
    const float den = bfr(__builtin_amdgcn_sqrtf(bfr(var + 1e-5f)));
    const float v = bfr(xo - bfr(mean)) * __builtin_amdgcn_rcpf(den);
    xn = bfpack(v, v);
  }
  PIN(xn);
  float* part = reinterpret_cast<float*>(lds + L_PART);
  float* pw = part + w * 128 + 32 * g + (c & 7);
  {
    f32x4 acc = {0.f, 0.f, 0.f, 0.f};
    sfor<0, PLAST>([&](auto N) {
      constexpr int n = N, code = PS.code[n];
      if constexpr (code < 16) { acc = sel_mfma(WQ[code], bcast1<code>(xn), mk, acc); PIN(acc); }
      else if constexpr (n >= LNPOS) { __builtin_amdgcn_s_waitcnt(vmcnt_imm(PW)); SB(); }
      if constexpr (n >= LNPOS) pissue(IC<n + PW + 1>{});
      SB();
    });
#pragma unroll
    for (int r = 0; r < 4; ++r) pw[8 * r] = acc[r];
    SB();
    kissue(IC<KA>{});
    SB();
  }
  __syncthreads();
  u32* vec = reinterpret_cast<u32*>(lds + L_VEC) + w * 64;
  auto sum_parts = [&]() {
    f32x2 p[NW];
#pragma unroll
    for (int u = 0; u < NW; ++u) p[u] = *reinterpret_cast<const f32x2*>(part + u * 128 + 2 * lane);
    const f32x2 s = ((p[0] + p[1]) + (p[2] + p[3])) + ((p[4] + p[5]) + (p[6] + p[7]));
    vec[lane] = bfpack(s[0], s[1]);
    __builtin_amdgcn_wave_barrier();
  };
  sum_parts();
  u32x4 q = *reinterpret_cast<const u32x4*>(vec + 4 * c);
  PIN(q);
  kissue(IC<KA + 1>{});
  SB();

  const int sl = (h & 7) * 4 + (h >> 3);
  const int nch = sl < 12 ? 6 : 5;
  const int f0 = sl < 12 ? 48 * sl : 576 + 40 * (sl - 12);
  const rsrc_t ro = mkrsrc(wo, NH * DH * DM * 2), r1 = mkrsrc(w1, DM * FF * 2), r2 = mkrsrc(w2, FF * DM * 2);
  char* wol = lds + L_WO + w * 16384;
  const u32 w1vo = (u32)(4 * (c >> 3) + g) * (FF * 2) + min(c & 7, nch - 1) * 16;
  const u32 wovo = (u32)(lane >> 3) * (DM * 2) + (lane & 7) * 16;
  const u32 wost = (u32)(lane >> 3) * 128 + (((lane & 7) ^ (((lane >> 4) & 1) << 1)) * 16);
  f32x4 oacc = {0.f, 0.f, 0.f, 0.f}, hacc = {0.f, 0.f, 0.f, 0.f};
  float sc[8];

  u32 due, pdt;
  auto repace = [&](float rem, float unit) {
    const u32 te = flag[1], now = clock32();
    if ((int)((now << 10) - due) > 0) due = now << 10;
    const float left = te ? fmaxf((float)(int)(te - now), 0.f) : 0.f;
    pdt = __builtin_amdgcn_readfirstlane((u32)(left * (2048.f * unit) * __builtin_amdgcn_rcpf(rem)));
  };
#define PACE_PRE(t) u32 now_; if constexpr ((t) % 2 == 0) now_ = clock32();
#define PACE_POST(t) if constexpr ((t) % 2 == 0) { while ((int)((now_ << 10) - due) < 0) now_ = clock32(); due += pdt; SB(); }

  auto ktile = [&](auto MODE, u32 i) {
    constexpr int mode = MODE;
    const u32 so = i * STB;
    const bool last = i == NT - 1;
    const rsrc_t rn = mkrsrc(last ? vb : kb, KVB);
    const u32 sn = last ? 0u : so + STB;
    if constexpr (mode == 3) repace(128.f + KW * 16.f * (float)(NT - i), KW);
    u32x4 sw[9];
    auto sld = [&](auto J, u32 gi) {
      constexpr int j = J;
      if constexpr (j % 3 == 0) sw[j] = bload<0>(r1, w1vo, (u32)(64 * wr + 4 * ((2 * gi + tr) & 15)) * (FF * 2) + f0 * 2);
      else sw[j] = bload<0>(ro, wovo, (u32)(h * DH + 16 * ((gi + b) & 7) + 8 * (j % 3 - 1)) * (DM * 2) + w * 128);
    };
    auto suse = [&](auto J, u32 gi) {
      constexpr int j = J;
      if constexpr (j % 3 == 0) {
        hacc = sel_mfma(sw[j], bcast2<0>(xn), mk, hacc); asm volatile("" : "+v"(hacc));
        xn = dppu<ROR + 14>(xn);
      } else *reinterpret_cast<u32x4*>(wol + ((gi + b) & 7) * 2048 + (j % 3 - 1) * 1024 + (wost ^ ((j % 3 - 1) * 64))) = sw[j];
    };
    float s[16];
    sfor<0, 16>([&](auto T) {
      constexpr int t = T, n = t + RD;
      PACE_PRE(mode == 3 ? t : 1)
      const u32x4 k = mode < 2 ? T0[(16 * mode + t) & 31] : R[t % RS];
      float d = dot2(k[0], q[0], 0.f);
      d = dot2(k[1], q[1], d); d = dot2(k[2], q[2], d); d = dot2(k[3], q[3], d);
      PIN(d);
      s[t] = d;
      PACE_POST(mode == 3 ? t : 1)
      if constexpr (mode < 2) {
        constexpr int j = 16 * mode + t;
        if constexpr (j == 8) kissue(IC<KA + 2>{});
        if constexpr (j == 15) kissue(IC<KA + 3>{});
        if constexpr (j + RD >= KB) kissue(IC<j + RD>{});
      } else {
        if constexpr (n >= 16) R[n % RS] = sload(rn, n - 16, sn); else R[n % RS] = sload(rk, n, so);
      }
      SB();
      if constexpr (mode == 3) {
        if constexpr (t % 5 == 0 && t < 15) sld(IC<t / 5>{}, i);
        if constexpr (t % 5 == 0 && t > 0) suse(IC<t / 5 - 1>{}, i);
      }
      if constexpr (mode == 2) {
        if constexpr (t < 9) sld(T, t / 3);
        if constexpr (t >= 7) suse(IC<t - 7>{}, (t - 7) / 3);
      }
      SB();
    });
#define TR_STEP(HALF, CTRL, BIT)                                                                \
  _Pragma("unroll") for (int j = 0; j < (HALF); ++j) {                                          \
    const float t1 = s[j] + dppf<CTRL>(s[j]), t2 = s[j + (HALF)] + dppf<CTRL>(s[j + (HALF)]);   \
    s[j] = (c & (BIT)) ? t2 : t1; }
    TR_STEP(8, XOR8, 8) TR_STEP(4, XOR7, 4) TR_STEP(2, XOR2, 2) TR_STEP(1, XOR1, 1)
#undef TR_STEP
#pragma unroll
    for (int k = 7; k > 0; --k) sc[k] = sc[k - 1];
    sc[0] = bfr(bfr(s[0]) * SCALE);
    PIN(sc[0]);
  };

  u32 pp[8], hh, svo;
  float cw, b1v;
  u32x4 WO[16], W2[6];
  f32x4 acc[4];
  float* hpart = reinterpret_cast<float*>(lds + L_HP);
  float* tmp = reinterpret_cast<float*>(lds + L_TMP) + w * 128;
  const bool diag = (g >> 1) == (c >> 3);
  auto vside = [&](auto J) {
    constexpr int j = J;
    if constexpr (j == 0) b1v = (float)b1[min(f0 + 4 * c + g, FF - 1)];
    if constexpr (j >= 1 && j < 7)
      W2[j - 1] = bload<0>(r2, (u32)(4 * (c >> 3) + g) * (DM * 2) + (c & 7) * 16, j - 1 < nch ? (u32)(f0 + 8 * (j - 1)) * (DM * 2) + w * 128 : OOB);
    if constexpr (j == 4) __syncthreads();
    if constexpr (j == 5) {
      float hs = 0.f;
#pragma unroll
      for (int u = 0; u < 16; ++u) hs += hpart[u * 64 + 4 * c + g];
      float hv = bfr(bfr(hs) + b1v);
      hv = hv > 0.f ? hv : 0.f;
      hh = (4 * c + g < 8 * nch) ? bfpack(hv * hv, hv * hv) : 0u;
      asm volatile("" : "+v"(hh));
      hacc = f32x4{0.f, 0.f, 0.f, 0.f};
    }
    if constexpr (j == 6) {
      f32x4 ml[NW / 2];
#pragma unroll
      for (int u = 0; u < NW / 2; ++u) ml[u] = *reinterpret_cast<const f32x4*>(lds + L_ML + u * 16);
      float M = -INFINITY, L = 0.f;
#pragma unroll
      for (int u = 0; u < NW; ++u) M = fmaxf(M, ml[u / 2][2 * (u & 1)]);
#pragma unroll
      for (int u = 0; u < NW; ++u) L = fmaf(__builtin_amdgcn_exp2f((ml[u / 2][2 * (u & 1)] - M) * LOG2E), ml[u / 2][2 * (u & 1) + 1], L);
      cw = __builtin_amdgcn_exp2f((cw - M) * LOG2E) * __builtin_amdgcn_rcpf(L);
      asm volatile("" : "+v"(cw));
    }
    if constexpr (j == 7) {
      svo = c < 4 ? (u32)c * 64 + g * 16 : OOB;
      asm volatile("" : "+v"(svo));
    }
    if constexpr (j >= 8 && j < 14) { hacc = sel_mfma(W2[j - 8], bcast2<j - 8>(hh), mk, hacc); asm volatile("" : "+v"(hacc)); }
    if constexpr (j == 14) {
#pragma unroll
      for (int r = 0; r < 4; ++r) {
        const float y = dppf<XOR8>(hacc[r]);
        const f32x2 t = swap32(y, y);
        tmp[(diag ? 0 : 64) + 32 * (g & 1) + 8 * r + (c & 7)] = hacc[r] + ((lane & 32) ? t[0] : t[1]);
      }
      __builtin_amdgcn_wave_barrier();
    }
    if constexpr (j == 15) {
#pragma unroll
      for (int nt = 0; nt < 4; ++nt) acc[nt] = *reinterpret_cast<const f32x4*>(tmp + 16 * nt + 4 * g);
    }
    {
      const int row = 8 * g + (c >> 2), swz = ((row >> 1) & 1) | (((row >> 3) & 1) << 1);
      const char* p = wol + (32 * (j & 3) + row) * 128 + (((j >> 2) ^ swz) * 32) + (c & 3) * 8;
      const u32x2 lo = __builtin_bit_cast(u32x2, __builtin_amdgcn_ds_read_tr16_b64_v4i16((LAS s16x4*)p));
      const u32x2 hi = __builtin_bit_cast(u32x2, __builtin_amdgcn_ds_read_tr16_b64_v4i16((LAS s16x4*)(p + 512)));
      WO[j] = u32x4{lo[0], lo[1], hi[0], hi[1]};
    }
  };
  auto vtile = [&](auto FIRST, u32 so) {
    repace((float)(KVB - so) * (16.f / STB), 1.f);
    const u32 p = pp[7];
#pragma unroll
    for (int k = 7; k > 0; --k) pp[k] = pp[k - 1];
    sfor<0, 16>([&](auto T) {
      constexpr int t = T, n = t + RD;
      PACE_PRE(t)
      oacc = sel_mfma(R[t % RS], bcast1<t>(p), mk, oacc);
      PIN(oacc);
      if (n < 16 || so + STB < KVB) { PACE_POST(t) }
      R[n % RS] = sload(rv, n, so);
      SB();
      if constexpr (FIRST) { vside(T); SB(); }
    });
  };

#pragma unroll
  for (int k = 0; k < 8; ++k) sc[k] = 0.f;
  ktile(IC<0>{}, 0u);
  ktile(IC<1>{}, 1u);
  ktile(IC<2>{}, 2u);
  due = clock32() << 10;
#pragma unroll 1
  for (u32 i = 3; i < NT; ++i) ktile(IC<3>{}, i);
  {
    float* hp = diag ? hpart + (2 * w + (g >> 1)) * 64 : tmp + (g >> 1) * 64;
#pragma unroll
    for (int r = 0; r < 4; ++r) hp[32 * (g & 1) + 8 * r + (c & 7)] = hacc[r];
    float m = fmaxf(fmaxf(fmaxf(sc[0], sc[1]), fmaxf(sc[2], sc[3])), fmaxf(fmaxf(sc[4], sc[5]), fmaxf(sc[6], sc[7])));
    m = wave_max(m);
    float l = 0.f;
#pragma unroll
    for (int k = 0; k < 8; ++k) { const float e = __builtin_amdgcn_exp2f((sc[k] - m) * LOG2E); pp[k] = bfpack(e, e); l += bflo(pp[k]); }
    l = wave_sum(l);
    *reinterpret_cast<f32x2*>(lds + L_ML + w * 8) = f32x2{m, l};
    cw = m;
    SB();
  }
  vtile(IC<1>{}, 0u);
#pragma unroll 1
  for (u32 so = STB; so < KVB; so += STB) vtile(IC<0>{}, so);

#pragma unroll
  for (int r = 0; r < 4; ++r) pw[8 * r] = oacc[r] * cw;
  __syncthreads();
  sum_parts();
  u32x4 ob[4];
#pragma unroll
  for (int ks = 0; ks < 4; ++ks) ob[ks] = *reinterpret_cast<const u32x4*>(vec + 16 * ks + 4 * g);
  SB();
#pragma unroll
  for (int ks = 0; ks < 4; ++ks)
#pragma unroll
    for (int nt = 0; nt < 4; ++nt) acc[nt] = mfma(WO[4 * nt + ks], ob[ks], acc[nt]);
  const f32x4 r01 = (c & 1) ? acc[1] : acc[0], r23 = (c & 1) ? acc[3] : acc[2];
  u32x4 ru = __builtin_bit_cast(u32x4, (c & 2) ? r23 : r01);
#pragma unroll
  for (int r = 0; r < 4; ++r) ru[r] = min(ru[r], SENT - 1);
  const u32 sso = (u32)h * 2048 + w * 256;
  bstore<AUX_SC1>(ru, rp, svo, sso);
  SB();
#pragma unroll
  for (int k = 0; k < RS; ++k) asm volatile("" :: "v"(R[k]));
  if (__builtin_amdgcn_readfirstlane(flag[0]) != 0) {
    __syncthreads();
    bstore<AUX_SC1>(ru, rp, svo, sso);
  }
}

extern "C" void kernel_launch(void* const* d_in, const int* in_sizes, int n_in, void* d_out, int out_size, void* d_ws,
                              size_t ws_size, hipStream_t stream) {
  fused_decode_block<<<NB * NH, NTHR, 0, stream>>>(
      (const __bf16*)d_in[1], (const __bf16*)d_in[3], (const __bf16*)d_in[0], (const __bf16*)d_in[2], (float*)d_ws,
      (const __bf16*)d_in[4], (const __bf16*)d_in[6], (const __bf16*)d_in[8], (const __bf16*)d_in[7], (const __bf16*)d_in[9],
      (__bf16*)d_out);
}
```
